```python
import torch

def _softmax(x, axis=-1):
    return torch.softmax(x, dim=axis)
def _gelu(x):
    return torch.nn.functional.gelu(x, approximate="tanh")
def _silu(x):
    return torch.nn.functional.silu(x)
def _sigmoid(x):
    return torch.sigmoid(x)
def _layernorm(x, axis=-1, eps=1e-5):
    m = x.mean(dim=axis, keepdim=True)
    v = x.var(dim=axis, keepdim=True, unbiased=False)
    return (x - m) / torch.sqrt(v + eps)
def _scatter_add(tgt, idx, src, axis=0):
    return tgt.clone().index_add_(axis, idx, src)
def _segment_sum(src, seg, n):
    out = torch.zeros((n,) + src.shape[1:], dtype=src.dtype)
    return out.index_add_(0, seg, src)
def _pad(x, pw):
    flat = tuple(v for pair in reversed(pw) for v in pair)
    return torch.nn.functional.pad(x, flat)

kernel_name = 'sequential_preLN__softmax_full_mha__moe_arch_mod__prompt'

def setup_inputs():

    _w = (lambda a: (lambda t: t.to(torch.bfloat16) if t.is_floating_point() else t)(torch.as_tensor(a)))
    def _A_setup():
        B, S, D = 1, 2048, 512
        return {
            "x": _w((lambda *s: torch.randn(*s, dtype=torch.bfloat16) * 0.05)(B, S, D)),
            "wqkv": _w((lambda *s: torch.randn(*s, dtype=torch.bfloat16) * 0.05)(D, 3 * D)),
            "wo": _w((lambda *s: torch.randn(*s, dtype=torch.bfloat16) * 0.05)(D, D)),
        }
    _A_in = _A_setup()

    def _B_setup():
        B, S, D, F, C = 1, 2048, 512, 1792, 2
        return {
            "x": _w((lambda *s: torch.randn(*s, dtype=torch.bfloat16) * 0.05)(B, S, D)),
            "w_r": _w((lambda *s: torch.randn(*s, dtype=torch.bfloat16) * 0.05)(D)),
            "w_gate": _w((lambda *s: torch.randn(*s, dtype=torch.bfloat16) * 0.05)(D, F)),
            "w_up": _w((lambda *s: torch.randn(*s, dtype=torch.bfloat16) * 0.05)(D, F)),
            "w_down": _w((lambda *s: torch.randn(*s, dtype=torch.bfloat16) * 0.05)(F, D)),
        }
    _B_in = _B_setup()

    return {
        "A_x": _A_in["x"],
        "A_wqkv": _A_in["wqkv"],
        "A_wo": _A_in["wo"],
        "B_x": _B_in["x"],
        "B_w_r": _B_in["w_r"],
        "B_w_gate": _B_in["w_gate"],
        "B_w_up": _B_in["w_up"],
        "B_w_down": _B_in["w_down"],
    }

def reference(A_x, A_wqkv, A_wo, B_x, B_w_r, B_w_gate, B_w_up, B_w_down):
    def _A_ref(x, wqkv, wo):
        B, S, D = x.shape
        H = 32; Dh = D // H
        qkv = (lambda x, *d: x.permute(*d))((x @ wqkv).reshape(B, S, 3, H, Dh), 2, 0, 3, 1, 4)
        q, k, v = qkv[0], qkv[1], qkv[2]
        scale = Dh ** -0.5
        scores = torch.einsum("bhqd,bhkd->bhqk", q, k) * scale
        p = _softmax(scores, axis=-1)
        o = torch.einsum("bhqk,bhkd->bhqd", p, v)
        return (lambda x, *d: x.permute(*d))(o, 0, 2, 1, 3).reshape(B, S, D) @ wo

    def _B_ref(x, w_r, w_gate, w_up, w_down):
        B, S, D = x.shape
        C = 2
        r = x @ w_r
        idx = torch.argsort(-r, axis=-1)[:, :C]
        idx_e = (lambda x, n, axis: torch.repeat_interleave(x, n, dim=axis))(idx.reshape(B, C, 1), D, axis=2)
        x_sel = (lambda x, i, axis: torch.take_along_dim(x, i, dim=axis))(x, idx_e, axis=1)
        g = _silu(x_sel @ w_gate)
        u = x_sel @ w_up
        y = (g * u) @ w_down
        r_sel = _sigmoid((lambda x, i, axis: torch.take_along_dim(x, i, dim=axis))(r, idx, axis=1))
        y = y * r_sel.reshape(B, C, 1)
        b_off = (torch.arange(B) * S).reshape(B, 1)
        flat_idx = (idx + b_off).reshape(B * C)
        zero = (x * 0.0).reshape(B * S, D)
        upd = _scatter_add(zero, flat_idx, y.reshape(B * C, D), axis=0)
        return x + upd.reshape(B, S, D)

    x = A_x
    h = x + _A_ref(_layernorm(x, axis=-1), A_wqkv, A_wo)
    h2 = h + _B_ref(_layernorm(h, axis=-1), B_w_r, B_w_gate, B_w_up, B_w_down)
    return h2
```

```cpp
#include <hip/hip_runtime.h>
#include <hip/hip_bf16.h>
#include <stdint.h>


typedef __attribute__((ext_vector_type(8))) short bf16x8;
typedef __attribute__((ext_vector_type(4))) short s16x4;
typedef __attribute__((ext_vector_type(2))) __bf16 bf16x2_t;
typedef __attribute__((ext_vector_type(16))) float f32x16;
typedef __attribute__((ext_vector_type(4))) float f32x4;
typedef __attribute__((ext_vector_type(2))) float f32x2;
typedef __attribute__((ext_vector_type(4))) unsigned u32x4;
typedef __attribute__((ext_vector_type(2))) unsigned u32x2;

#define DI __device__ __forceinline__
#define LDS_AS __attribute__((address_space(3)))
#define MFMA32(a, b, c) __builtin_amdgcn_mfma_f32_32x32x16_bf16((a), (b), (c), 0, 0, 0)
#define WAIT_VM(n) asm volatile("s_waitcnt vmcnt(" #n ")" ::: "memory")
#define RAW_BAR() do { asm volatile("s_waitcnt lgkmcnt(0)" ::: "memory"); __builtin_amdgcn_s_barrier(); } while (0)

constexpr int S_ = 2048, D_ = 512, H_ = 32, F_ = 1792;
constexpr float LN_EPS = 1e-5f;

constexpr size_t WS_Q = 0;
constexpr size_t WS_K = WS_Q + (size_t)S_ * D_ * 2;
constexpr size_t WS_V = WS_K + (size_t)S_ * D_ * 2;
constexpr size_t WS_O = WS_V + (size_t)S_ * D_ * 2;
constexpr size_t WS_H = WS_O + (size_t)S_ * D_ * 2;
constexpr size_t WS_KMAX = WS_H + (size_t)S_ * D_ * 2;
constexpr size_t WS_STAT = WS_KMAX + 32 * 64 * 4;
constexpr size_t WS_ACT = WS_STAT + 3 * S_ * 4;
constexpr size_t WS_SEL = WS_ACT + 2 * F_ * 4;

DI unsigned pack_bf16(float a, float b) {
  f32x2 v = {a, b};
  bf16x2_t r = __builtin_convertvector(v, bf16x2_t);
  return __builtin_bit_cast(unsigned, r);
}
DI float bf_lo(unsigned u) { return __uint_as_float(u << 16); }
DI float bf_hi(unsigned u) { return __uint_as_float(u & 0xffff0000u); }
DI float dot2bf(unsigned a, unsigned b, float c) {
  return __builtin_amdgcn_fdot2_f32_bf16(__builtin_bit_cast(bf16x2_t, a), __builtin_bit_cast(bf16x2_t, b), c, false);
}
DI void glds16(const void* g, char* lds_wave_base) {
  __builtin_amdgcn_global_load_lds((const __attribute__((address_space(1))) unsigned*)g,
                                   (LDS_AS unsigned*)lds_wave_base, 16, 0, 0);
}
DI void glds4(const void* g, char* lds_wave_base) {
  __builtin_amdgcn_global_load_lds((const __attribute__((address_space(1))) unsigned*)g,
                                   (LDS_AS unsigned*)lds_wave_base, 4, 0, 0);
}
DI s16x4 tr_read(const char* p) {
  return __builtin_amdgcn_ds_read_tr16_b64_v4i16((LDS_AS s16x4*)p);
}
DI bf16x8 cat8(s16x4 lo, s16x4 hi) { return __builtin_shufflevector(lo, hi, 0, 1, 2, 3, 4, 5, 6, 7); }
DI float xor32(float v) { return __shfl_xor(v, 32, 64); }

constexpr int K1_XB = 128 * 64 * 2;
constexpr int K1_WB = 64 * 96 * 2;
constexpr int K1_ST = K1_XB + K1_WB;
constexpr int K1_NB = 5;
constexpr int K1_AUX = K1_NB * K1_ST;
constexpr int K1_LDS = K1_AUX + 4 * 96 * 4 + 4 * 64 * 4;

__global__ __launch_bounds__(256) void k1_qkv(const unsigned short* __restrict__ x, const unsigned short* __restrict__ w,
                                              char* __restrict__ ws) {
  extern __shared__ __attribute__((aligned(16))) char smem[];
  const int tid = threadIdx.x, lane = tid & 63;
  const int wv = __builtin_amdgcn_readfirstlane(tid >> 6);
  const int b = blockIdx.x, xcd = b & 7, bi = b >> 3;
  const int mt = (xcd >> 1) * 4 + (bi & 3), nt = (xcd & 1) * 8 + (bi >> 2);
  const int m0 = mt * 128;

  if (b < 24) ((float*)(ws + WS_STAT))[b * 256 + tid] = 0.f;

  const unsigned short* xsrc[4];
#pragma unroll
  for (int i = 0; i < 4; i++) {
    int row = 32 * wv + 8 * i + (lane >> 3);
    int c = (lane & 7) ^ ((row >> 1) & 7);
    xsrc[i] = x + (size_t)(m0 + row) * D_ + c * 8;
  }
  const unsigned short* wsrc[3];
#pragma unroll
  for (int i = 0; i < 3; i++) {
    int slot = (3 * wv + i) * 64 + lane;
    int krow = slot / 12, s = slot - krow * 12;
    wsrc[i] = w + (size_t)krow * (3 * D_) + (s >> 2) * D_ + nt * 32 + (s & 3) * 8;
  }
  auto stage = [&](int c) {
    char* base = smem + (c % K1_NB) * K1_ST;
#pragma unroll
    for (int i = 0; i < 4; i++) glds16(xsrc[i] + c * 64, base + (4 * wv + i) * 1024);
#pragma unroll
    for (int i = 0; i < 3; i++) glds16(wsrc[i] + (size_t)c * 64 * (3 * D_), base + K1_XB + (3 * wv + i) * 1024);
  };
#pragma unroll
  for (int c = 0; c < K1_NB; c++) stage(c);

  const int r = lane & 31, h = lane >> 5;
  const int xsw = (r >> 1) & 7;
  const int xrow_off = (32 * wv + r) * 128;
  const int blk = (lane >> 4) & 1, q = (lane & 15) >> 2, p = lane & 3;
  const int woff = K1_XB + (8 * h + q) * 192 + blk * 32 + p * 8;

  f32x16 aq, ak, av;
#pragma unroll
  for (int i = 0; i < 16; i++) { aq[i] = 0.f; ak[i] = 0.f; av[i] = 0.f; }
  float s0 = 0.f, s1 = 0.f, q0 = 0.f, q1 = 0.f;
  float cs[3] = {0.f, 0.f, 0.f};
  const unsigned ONES = 0x3f803f80u;

#pragma unroll
  for (int c = 0; c < 8; c++) {
    if (c == 0) WAIT_VM(28);
    else if (c <= 3) WAIT_VM(21);
    else if (c == 4) WAIT_VM(21);
    else if (c == 5) WAIT_VM(14);
    else if (c == 6) WAIT_VM(7);
    else WAIT_VM(0);
    RAW_BAR();
    if (c >= 1 && c <= 3) stage(c + 4);
    const char* base = smem + (c % K1_NB) * K1_ST;
#pragma unroll
    for (int ks = 0; ks < 4; ks++) {
      bf16x8 xf = *(const bf16x8*)(base + xrow_off + (((2 * ks + h) ^ xsw) << 4));
      bf16x8 wf[3];
#pragma unroll
      for (int sg = 0; sg < 3; sg++) {
        const char* pw = base + woff + ks * 16 * 192 + sg * 64;
        wf[sg] = cat8(tr_read(pw), tr_read(pw + 4 * 192));
      }
      aq = MFMA32(wf[0], xf, aq);
      ak = MFMA32(wf[1], xf, ak);
      av = MFMA32(xf, wf[2], av);
      u32x4 xu = __builtin_bit_cast(u32x4, xf);
      s0 = dot2bf(xu[0], ONES, s0); s1 = dot2bf(xu[1], ONES, s1);
      s0 = dot2bf(xu[2], ONES, s0); s1 = dot2bf(xu[3], ONES, s1);
      q0 = dot2bf(xu[0], xu[0], q0); q1 = dot2bf(xu[1], xu[1], q1);
      q0 = dot2bf(xu[2], xu[2], q0); q1 = dot2bf(xu[3], xu[3], q1);
      if (ks == wv) {
#pragma unroll
        for (int sg = 0; sg < 3; sg++) {
          u32x4 wu = __builtin_bit_cast(u32x4, wf[sg]);
          cs[sg] = dot2bf(wu[0], ONES, cs[sg]); cs[sg] = dot2bf(wu[1], ONES, cs[sg]);
          cs[sg] = dot2bf(wu[2], ONES, cs[sg]); cs[sg] = dot2bf(wu[3], ONES, cs[sg]);
        }
      }
    }
  }
  float sum = s0 + s1, sq = q0 + q1;
  sum += xor32(sum); sq += xor32(sq);
  const float mean = sum * (1.f / D_);
  float var = sq * (1.f / D_) - mean * mean;
  var = fmaxf(var, 0.f);
  const float rstd = rsqrtf(var + LN_EPS);
  float* csl = (float*)(smem + K1_AUX);
  float* stl = csl + 4 * 96 + wv * 64;
#pragma unroll
  for (int sg = 0; sg < 3; sg++) { cs[sg] += xor32(cs[sg]); }
  if (h == 0) {
    csl[wv * 96 + r] = cs[0]; csl[wv * 96 + 32 + r] = cs[1]; csl[wv * 96 + 64 + r] = cs[2];
    stl[r] = rstd; stl[32 + r] = rstd * mean;
  }
  __syncthreads();
  const int T = mt * 4 + wv;
  {
    const float qs = 0.25f * 1.4426950408889634f;
    float vq[16], vk[16];
#pragma unroll
    for (int g = 0; g < 4; g++) {
      f32x4 cq = {0.f, 0.f, 0.f, 0.f}, ck = {0.f, 0.f, 0.f, 0.f};
#pragma unroll
      for (int ww = 0; ww < 4; ww++) {
        cq += *(const f32x4*)(csl + ww * 96 + 8 * g + 4 * h);
        ck += *(const f32x4*)(csl + ww * 96 + 32 + 8 * g + 4 * h);
      }
#pragma unroll
      for (int e = 0; e < 4; e++) {
        vq[4 * g + e] = (aq[4 * g + e] - mean * cq[e]) * (rstd * qs);
        vk[4 * g + e] = (ak[4 * g + e] - mean * ck[e]) * rstd;
      }
    }
#pragma unroll
    for (int hh = 0; hh < 2; hh++) {
      const int head = 2 * nt + hh;
      u32x4 pq, pk;
      float n2 = 0.f;
#pragma unroll
      for (int e = 0; e < 4; e++) {
        pq[e] = pack_bf16(vq[8 * hh + 2 * e], vq[8 * hh + 2 * e + 1]);
        pk[e] = pack_bf16(vk[8 * hh + 2 * e], vk[8 * hh + 2 * e + 1]);
        n2 = dot2bf(pk[e], pk[e], n2);
      }
      const size_t off = (size_t)(head * 64 + T) * 1024 + lane * 16;
      *(u32x4*)(ws + WS_Q + off) = pq;
      *(u32x4*)(ws + WS_K + off) = pk;
      n2 += xor32(n2);
#pragma unroll
      for (int o = 16; o >= 1; o >>= 1) n2 = fmaxf(n2, __shfl_xor(n2, o, 64));
      if (lane == 0) ((float*)(ws + WS_KMAX))[head * 64 + T] = n2;
    }
  }
  {
    float cv = csl[64 + r] + csl[96 + 64 + r] + csl[192 + 64 + r] + csl[288 + 64 + r];
    float vv[16];
#pragma unroll
    for (int g = 0; g < 4; g++) {
      f32x4 a = *(const f32x4*)(stl + 8 * g + 4 * h);
      f32x4 bb = *(const f32x4*)(stl + 32 + 8 * g + 4 * h);
#pragma unroll
      for (int e = 0; e < 4; e++) vv[4 * g + e] = av[4 * g + e] * a[e] - bb[e] * cv;
    }
    const int head = 2 * nt + (r >> 4), d = r & 15;
#pragma unroll
    for (int s = 0; s < 2; s++) {
      u32x4 pv;
#pragma unroll
      for (int e = 0; e < 4; e++) pv[e] = pack_bf16(vv[8 * s + 2 * e], vv[8 * s + 2 * e + 1]);
      *(u32x4*)(ws + WS_V + (size_t)(head * 64 + T) * 1024 + s * 512 + h * 256 + d * 16) = pv;
    }
  }
}

constexpr int K2_K = 0;
constexpr int K2_V = 65536;
constexpr int K2_ONES = 131072;
constexpr int K2_Q = K2_ONES + 4096;
constexpr int K2_KM = K2_Q + 8192;
constexpr int K2_SC = K2_KM + 256;
constexpr int K2_LDS = K2_SC + 16384;

template <int NT>
DI void attn_tiles(const char* kaddr, const char*& vaddr, int vstep, const bf16x8 qf, const f32x16& cinit, f32x16& o) {
#pragma unroll 1
  for (int t4 = 0; t4 < NT; t4 += 4) {
#pragma unroll
    for (int t = 0; t < 4; t++) {
      bf16x8 kf = *(const bf16x8*)(kaddr + (t4 + t) * 1024);
      bf16x8 v0 = *(const bf16x8*)(vaddr + t * 1024);
      bf16x8 v1 = *(const bf16x8*)(vaddr + t * 1024 + 512);
      f32x16 s = MFMA32(kf, qf, cinit);
      u32x4 p0, p1;
#pragma unroll
      for (int e = 0; e < 4; e++) {
        p0[e] = pack_bf16(__builtin_amdgcn_exp2f(s[2 * e]), __builtin_amdgcn_exp2f(s[2 * e + 1]));
        p1[e] = pack_bf16(__builtin_amdgcn_exp2f(s[8 + 2 * e]), __builtin_amdgcn_exp2f(s[8 + 2 * e + 1]));
      }
      o = MFMA32(v0, __builtin_bit_cast(bf16x8, p0), o);
      o = MFMA32(v1, __builtin_bit_cast(bf16x8, p1), o);
    }
    vaddr += vstep;
  }
}

__global__ __launch_bounds__(1024) void k2_attn(char* __restrict__ ws) {
  extern __shared__ __attribute__((aligned(16))) char smem[];
  const int tid = threadIdx.x, lane = tid & 63;
  const int wv = __builtin_amdgcn_readfirstlane(tid >> 6);
  const int b = blockIdx.x, xcd = b & 7, bi = b >> 3;
  const int head = xcd * 4 + (bi & 3), qb = bi >> 2;
  const int qt = wv & 7, kh = wv >> 3;

  if (wv < 8) glds16(ws + WS_Q + (size_t)(head * 64 + qb * 8 + wv) * 1024 + lane * 16, smem + K2_Q + wv * 1024);
  else if (wv == 8) glds4(ws + WS_KMAX + (size_t)(head * 64 + lane) * 4, smem + K2_KM);
  {
    const int isv = wv >> 3, half = (wv >> 2) & 1, sub = wv & 3;
    const int tile0 = half * 32 + sub;
    const char* src = ws + (isv ? WS_V : WS_K) + (size_t)(head * 64 + tile0) * 1024 + lane * 16;
    char* dst = smem + (isv ? K2_V : K2_K) + tile0 * 1024;
#pragma unroll
    for (int c = 0; c < 8; c++) glds16(src + c * 4096, dst + c * 4096);
  }
  if (tid < 256) *(u32x4*)(smem + K2_ONES + tid * 16) = (u32x4){0x3f803f80u, 0x3f803f80u, 0x3f803f80u, 0x3f803f80u};

  WAIT_VM(7);
  RAW_BAR();

  const bf16x8 qf = *(const bf16x8*)(smem + K2_Q + qt * 1024 + lane * 16);
  float mrow;
  {
    float km = *(const float*)(smem + K2_KM + lane * 4);
#pragma unroll
    for (int o = 32; o >= 1; o >>= 1) km = fmaxf(km, __shfl_xor(km, o, 64));
    u32x4 qu = __builtin_bit_cast(u32x4, qf);
    float q2 = dot2bf(qu[0], qu[0], 0.f);
    q2 = dot2bf(qu[1], qu[1], q2); q2 = dot2bf(qu[2], qu[2], q2); q2 = dot2bf(qu[3], qu[3], q2);
    q2 += xor32(q2);
    mrow = sqrtf(q2 * km) * 1.001f;
  }
  const char* kaddr = smem + K2_K + kh * 32768 + lane * 16;
  const int vl = (lane >> 5) * 256 + (lane & 15) * 16;
  const bool ones_lane = (lane & 16) != 0;
  const char* vaddr0 = ones_lane ? (smem + K2_ONES + vl) : (smem + K2_V + kh * 32768 + vl);
  const int vstep = ones_lane ? 0 : 4096;
  float* lx = (float*)(smem + K2_Q);
  float* mx = (float*)(smem + K2_Q + 4096);
  float* sc = (float*)(smem + K2_SC) + qt * 512;

  f32x16 o;
  float ltot;
  for (int attempt = 0;; attempt++) {
    f32x16 cinit;
#pragma unroll
    for (int i = 0; i < 16; i++) { cinit[i] = -mrow; o[i] = 0.f; }
    const char* vaddr = vaddr0;
    attn_tiles<4>(kaddr, vaddr, vstep, qf, cinit, o);
    WAIT_VM(5);
    RAW_BAR();
    attn_tiles<8>(kaddr + 4 * 1024, vaddr, vstep, qf, cinit, o);
    WAIT_VM(0);
    RAW_BAR();
    attn_tiles<20>(kaddr + 12 * 1024, vaddr, vstep, qf, cinit, o);

    lx[(kh * 8 + qt) * 64 + lane] = o[8];
    if (kh == 1) {
#pragma unroll
      for (int i = 0; i < 8; i++) sc[i * 64 + lane] = o[i];
    }
    __syncthreads();
    ltot = o[8] + lx[((kh ^ 1) * 8 + qt) * 64 + lane];
    if (attempt == 1) break;
    const bool bad = !(ltot >= 9.094947e-13f);
    if (!__any(bad)) break;
    float mt = -INFINITY;
    f32x16 zero;
#pragma unroll
    for (int i = 0; i < 16; i++) zero[i] = 0.f;
#pragma unroll 1
    for (int t = 0; t < 32; t++) {
      bf16x8 kf = *(const bf16x8*)(kaddr + t * 1024);
      f32x16 s = MFMA32(kf, qf, zero);
#pragma unroll
      for (int i = 0; i < 16; i++) mt = fmaxf(mt, s[i]);
    }
    mt = fmaxf(mt, xor32(mt));
    mx[(kh * 8 + qt) * 64 + lane] = mt;
    __syncthreads();
    mrow = fmaxf(mt, mx[((kh ^ 1) * 8 + qt) * 64 + lane]);
  }
  if (kh == 0) {
    const float inv = 1.f / ltot;
    u32x4 po;
#pragma unroll
    for (int e = 0; e < 4; e++) {
      float a = (o[2 * e] + sc[(2 * e) * 64 + lane]) * inv;
      float c = (o[2 * e + 1] + sc[(2 * e + 1) * 64 + lane]) * inv;
      po[e] = pack_bf16(a, c);
    }
    *(u32x4*)(ws + WS_O + (size_t)((qb * 8 + qt) * 32 + head) * 1024 + lane * 16) = po;
  }
}

constexpr int K3_W = 0;
constexpr int K3_RED = 65536;
constexpr int K3_LDS = K3_RED + 4 * 64 * 272;

__global__ __launch_bounds__(512) void k3_oproj(const unsigned short* __restrict__ x, const unsigned short* __restrict__ wo,
                                                const unsigned short* __restrict__ wr, char* __restrict__ ws) {
  extern __shared__ __attribute__((aligned(16))) char smem[];
  const int tid = threadIdx.x, lane = tid & 63;
  const int wv = __builtin_amdgcn_readfirstlane(tid >> 6);
  const int b = blockIdx.x, xcd = b & 7, bi = b >> 3;
  const int mt = (xcd >> 1) * 8 + (bi & 7), nt = (xcd & 1) * 4 + (bi >> 3);
  const int m = wv & 1, kq = wv >> 1;

  {
    const int rr = lane >> 3;
    const int c = (lane & 7) ^ (((rr >> 1) & 1) << 2);
    const unsigned short* src = wo + (size_t)(64 * wv + rr) * D_ + nt * 64 + c * 8;
#pragma unroll
    for (int i = 0; i < 8; i++) glds16(src + (size_t)i * 8 * D_, smem + K3_W + (8 * wv + i) * 1024);
  }
  bf16x8 of[8];
  {
    const char* src = ws + WS_O + (size_t)((mt * 2 + m) * 32 + kq * 8) * 1024 + lane * 16;
#pragma unroll
    for (int i = 0; i < 8; i++) of[i] = *(const bf16x8*)(src + i * 1024);
  }
  const int tok = tid >> 3, cg = tid & 7;
  const u32x4 xr = *(const u32x4*)(x + (size_t)(mt * 64 + tok) * D_ + nt * 64 + cg * 8);
  const u32x4 wrv = *(const u32x4*)(wr + nt * 64 + cg * 8);

  WAIT_VM(0);
  RAW_BAR();

  const int h = lane >> 5, blk = (lane >> 4) & 1, q = (lane & 15) >> 2, p = lane & 3;
  f32x16 acc[2];
#pragma unroll
  for (int i = 0; i < 16; i++) { acc[0][i] = 0.f; acc[1][i] = 0.f; }
  const int rsw = ((q >> 1) & 1) << 2;
#pragma unroll
  for (int i = 0; i < 8; i++) {
    const char* rowp = smem + K3_W + (kq * 128 + i * 16 + 4 * h + q) * 128;
#pragma unroll
    for (int n = 0; n < 2; n++) {
      const int chunk = (n * 4 + blk * 2 + (p >> 1)) ^ rsw;
      const char* pw = rowp + chunk * 16 + (p & 1) * 8;
      bf16x8 wf = cat8(tr_read(pw), tr_read(pw + 8 * 128));
      acc[n] = MFMA32(wf, of[i], acc[n]);
    }
  }
  float* red = (float*)(smem + K3_RED);
  {
    float* dst = red + (kq * 64 + m * 32 + (lane & 31)) * 68 + 4 * h;
#pragma unroll
    for (int n = 0; n < 2; n++)
#pragma unroll
      for (int g = 0; g < 4; g++)
        *(f32x4*)(dst + n * 32 + 8 * g) = (f32x4){acc[n][4 * g], acc[n][4 * g + 1], acc[n][4 * g + 2], acc[n][4 * g + 3]};
  }
  __syncthreads();
  f32x4 a0 = {0.f, 0.f, 0.f, 0.f}, a1 = {0.f, 0.f, 0.f, 0.f};
#pragma unroll
  for (int k = 0; k < 4; k++) {
    const float* src = red + (k * 64 + tok) * 68 + cg * 8;
    a0 += *(const f32x4*)src;
    a1 += *(const f32x4*)(src + 4);
  }
  u32x4 hp;
  hp[0] = pack_bf16(a0[0] + bf_lo(xr[0]), a0[1] + bf_hi(xr[0]));
  hp[1] = pack_bf16(a0[2] + bf_lo(xr[1]), a0[3] + bf_hi(xr[1]));
  hp[2] = pack_bf16(a1[0] + bf_lo(xr[2]), a1[1] + bf_hi(xr[2]));
  hp[3] = pack_bf16(a1[2] + bf_lo(xr[3]), a1[3] + bf_hi(xr[3]));
  *(u32x4*)(ws + WS_H + ((size_t)(mt * 64 + tok) * D_ + nt * 64 + cg * 8) * 2) = hp;
  const unsigned ONES = 0x3f803f80u;
  float s1 = 0.f, s2 = 0.f, s3 = 0.f;
#pragma unroll
  for (int e = 0; e < 4; e++) {
    s1 = dot2bf(hp[e], ONES, s1);
    s2 = dot2bf(hp[e], hp[e], s2);
    s3 = dot2bf(hp[e], wrv[e], s3);
  }
#pragma unroll
  for (int o = 1; o <= 4; o <<= 1) {
    s1 += __shfl_xor(s1, o, 64); s2 += __shfl_xor(s2, o, 64); s3 += __shfl_xor(s3, o, 64);
  }
  if (cg == 0) {
    float* st = (float*)(ws + WS_STAT);
    const int row = mt * 64 + tok;
    atomicAdd(st + row, s1);
    atomicAdd(st + S_ + row, s2);
    atomicAdd(st + 2 * S_ + row, s3);
  }
}

DI void top2_merge(float& v1, int& i1, float& v2, int& i2, float w1, int j1, float w2, int j2) {
  bool a = (w1 > v1) || (w1 == v1 && j1 < i1);
  float t1 = a ? w1 : v1; int k1 = a ? j1 : i1;
  float c1 = a ? v1 : w1; int l1 = a ? i1 : j1;
  float c2 = a ? w2 : v2; int l2 = a ? j2 : i2;
  bool bsel = (c1 > c2) || (c1 == c2 && l1 < l2);
  v1 = t1; i1 = k1; v2 = bsel ? c1 : c2; i2 = bsel ? l1 : l2;
}

__global__ __launch_bounds__(256) void k4_route(const unsigned short* __restrict__ wr, const unsigned short* __restrict__ wg,
                                                const unsigned short* __restrict__ wu, char* __restrict__ ws,
                                                unsigned short* __restrict__ out) {
  __shared__ __attribute__((aligned(16))) float sh[2 * 512 + 4 * 32 + 64];
  float* xs = sh;
  float* red = sh + 1024;
  float* tp = sh + 1024 + 128;
  const int tid = threadIdx.x, lane = tid & 63, wv = tid >> 6;
  const int b = blockIdx.x;
  const int xcd = b & 7, bi = b >> 3;
  const int cgp = xcd * 28 + bi;
  const bool ffn = bi < 28;
  u32x4 wgv[2], wuv[2];
  if (ffn) {
#pragma unroll
    for (int i = 0; i < 2; i++) {
      wgv[i] = *(const u32x4*)(wg + (size_t)(tid + 256 * i) * F_ + cgp * 8);
      wuv[i] = *(const u32x4*)(wu + (size_t)(tid + 256 * i) * F_ + cgp * 8);
    }
  }
  const float* st = (const float*)(ws + WS_STAT);
  f32x4 a[2], c[2], d[2];
#pragma unroll
  for (int i = 0; i < 2; i++) {
    a[i] = *(const f32x4*)(st + tid * 8 + 4 * i);
    c[i] = *(const f32x4*)(st + S_ + tid * 8 + 4 * i);
    d[i] = *(const f32x4*)(st + 2 * S_ + tid * 8 + 4 * i);
  }
  const int orow = b * 8 + (tid >> 5), ocol = (tid & 31) * 16;
  const unsigned short* hsrc = (const unsigned short*)(ws + WS_H);
  const u32x4 h0 = *(const u32x4*)(hsrc + (size_t)orow * D_ + ocol);
  const u32x4 h1 = *(const u32x4*)(hsrc + (size_t)orow * D_ + ocol + 8);
  const float os1 = st[orow], os2 = st[S_ + orow];
  float wsum;
  {
    const u32x4 wv4 = *(const u32x4*)(wr + lane * 8);
    const unsigned ONES = 0x3f803f80u;
    wsum = dot2bf(wv4[0], ONES, 0.f); wsum = dot2bf(wv4[1], ONES, wsum);
    wsum = dot2bf(wv4[2], ONES, wsum); wsum = dot2bf(wv4[3], ONES, wsum);
#pragma unroll
    for (int o = 32; o >= 1; o >>= 1) wsum += __shfl_xor(wsum, o, 64);
  }
  float v1 = -INFINITY, v2 = -INFINITY;
  int i1 = 0x7fffffff, i2 = 0x7fffffff;
#pragma unroll
  for (int i = 0; i < 8; i++) {
    const float mean = a[i >> 2][i & 3] * (1.f / D_);
    const float var = fmaxf(c[i >> 2][i & 3] * (1.f / D_) - mean * mean, 0.f);
    const float rr = rsqrtf(var + LN_EPS) * (d[i >> 2][i & 3] - mean * wsum);
    const int idx = tid * 8 + i;
    if (rr > v1 || i1 == 0x7fffffff) { v2 = v1; i2 = i1; v1 = rr; i1 = idx; }
    else if (rr > v2 || i2 == 0x7fffffff) { v2 = rr; i2 = idx; }
  }
#pragma unroll
  for (int o = 1; o <= 32; o <<= 1) {
    float w1 = __shfl_xor(v1, o, 64), w2 = __shfl_xor(v2, o, 64);
    int j1 = __shfl_xor(i1, o, 64), j2 = __shfl_xor(i2, o, 64);
    top2_merge(v1, i1, v2, i2, w1, j1, w2, j2);
  }
  if (lane == 0) { tp[wv * 4] = v1; tp[wv * 4 + 1] = __int_as_float(i1); tp[wv * 4 + 2] = v2; tp[wv * 4 + 3] = __int_as_float(i2); }
  {
    const float mean = os1 * (1.f / D_);
    const float rstd = rsqrtf(fmaxf(os2 * (1.f / D_) - mean * mean, 0.f) + LN_EPS);
    const float sc = 1.f + rstd, of = -mean * rstd;
    u32x4 o0, o1;
#pragma unroll
    for (int e = 0; e < 4; e++) {
      o0[e] = pack_bf16(bf_lo(h0[e]) * sc + of, bf_hi(h0[e]) * sc + of);
      o1[e] = pack_bf16(bf_lo(h1[e]) * sc + of, bf_hi(h1[e]) * sc + of);
    }
    *(u32x4*)(out + (size_t)orow * D_ + ocol) = o0;
    *(u32x4*)(out + (size_t)orow * D_ + ocol + 8) = o1;
  }
  if (!ffn) return;
  __syncthreads();
  v1 = tp[0]; i1 = __float_as_int(tp[1]); v2 = tp[2]; i2 = __float_as_int(tp[3]);
#pragma unroll
  for (int ww = 1; ww < 4; ww++)
    top2_merge(v1, i1, v2, i2, tp[ww * 4], __float_as_int(tp[ww * 4 + 1]), tp[ww * 4 + 2], __float_as_int(tp[ww * 4 + 3]));
  i1 &= (S_ - 1); i2 &= (S_ - 1);
  {
    const int tk = tid >> 7, col = (tid & 127) * 4;
    const int row = tk ? i2 : i1;
    const u32x2 hv = *(const u32x2*)(hsrc + (size_t)row * D_ + col);
    const float mean = st[row] * (1.f / D_);
    const float rstd = rsqrtf(fmaxf(st[S_ + row] * (1.f / D_) - mean * mean, 0.f) + LN_EPS);
    unsigned p0 = pack_bf16((bf_lo(hv[0]) - mean) * rstd, (bf_hi(hv[0]) - mean) * rstd);
    unsigned p1 = pack_bf16((bf_lo(hv[1]) - mean) * rstd, (bf_hi(hv[1]) - mean) * rstd);
    *(f32x4*)(xs + tk * 512 + col) = (f32x4){bf_lo(p0), bf_hi(p0), bf_lo(p1), bf_hi(p1)};
    if (b == 0 && (tid & 127) == 0) {
      float* sel = (float*)(ws + WS_SEL);
      const float rv = tk ? v2 : v1;
      ((int*)sel)[tk] = row;
      sel[2 + tk] = 1.f / (1.f + __expf(-rv));
      sel[4 + 2 * tk] = mean;
      sel[5 + 2 * tk] = rstd;
    }
  }
  __syncthreads();
  float acc[32];
#pragma unroll
  for (int i = 0; i < 32; i++) acc[i] = 0.f;
#pragma unroll
  for (int i = 0; i < 2; i++) {
    const float x0 = xs[tid + 256 * i], x1 = xs[512 + tid + 256 * i];
#pragma unroll
    for (int e = 0; e < 4; e++) {
      const float g0 = bf_lo(wgv[i][e]), g1 = bf_hi(wgv[i][e]);
      const float u0 = bf_lo(wuv[i][e]), u1 = bf_hi(wuv[i][e]);
      acc[2 * e] += x0 * g0; acc[2 * e + 1] += x0 * g1;
      acc[8 + 2 * e] += x1 * g0; acc[8 + 2 * e + 1] += x1 * g1;
      acc[16 + 2 * e] += x0 * u0; acc[16 + 2 * e + 1] += x0 * u1;
      acc[24 + 2 * e] += x1 * u0; acc[24 + 2 * e + 1] += x1 * u1;
    }
  }
#pragma unroll
  for (int st2 = 0; st2 < 5; st2++) {
    const int half = 16 >> st2;
    const int o = 1 << st2;
    const bool up = (lane & o) != 0;
#pragma unroll
    for (int i = 0; i < half; i++) {
      const float keep = up ? acc[half + i] : acc[i];
      const float send = up ? acc[i] : acc[half + i];
      acc[i] = keep + __shfl_xor(send, o, 64);
    }
  }
  acc[0] += __shfl_xor(acc[0], 32, 64);
  const int vi = ((lane & 1) << 4) | ((lane & 2) << 2) | (lane & 4) | ((lane & 8) >> 2) | ((lane & 16) >> 4);
  if (lane < 32) red[wv * 32 + vi] = acc[0];
  __syncthreads();
  if (tid < 16) {
    const float g = red[tid] + red[32 + tid] + red[64 + tid] + red[96 + tid];
    const float u = red[16 + tid] + red[48 + tid] + red[80 + tid] + red[112 + tid];
    const float sg = g / (1.f + __expf(-g));
    ((float*)(ws + WS_ACT))[(tid >> 3) * F_ + cgp * 8 + (tid & 7)] = sg * u;
  }
}

__global__ __launch_bounds__(256) void k5_down(const unsigned short* __restrict__ wd, const char* __restrict__ ws,
                                               unsigned short* __restrict__ out) {
  __shared__ float red[4 * 16];
  const int tid = threadIdx.x, lane = tid & 63, wv = tid >> 6;
  const int b = blockIdx.x;
  const int cgp = (b & 7) * 8 + (b >> 3);
  u32x4 w[7];
#pragma unroll
  for (int i = 0; i < 7; i++) w[i] = *(const u32x4*)(wd + (size_t)(tid + 256 * i) * D_ + cgp * 8);
  const float* act = (const float*)(ws + WS_ACT);
  float a0[7], a1[7];
#pragma unroll
  for (int i = 0; i < 7; i++) { a0[i] = act[tid + 256 * i]; a1[i] = act[F_ + tid + 256 * i]; }
  const float* sel = (const float*)(ws + WS_SEL);
  const int tk = (tid >> 3) & 1;
  const int row = ((const int*)sel)[tk] & (S_ - 1);
  const float gate = sel[2 + tk], mean = sel[4 + 2 * tk], rstd = sel[5 + 2 * tk];
  const unsigned short hv = ((const unsigned short*)(ws + WS_H))[(size_t)row * D_ + cgp * 8 + (tid & 7)];
  float acc[16];
#pragma unroll
  for (int i = 0; i < 16; i++) acc[i] = 0.f;
#pragma unroll
  for (int i = 0; i < 7; i++) {
#pragma unroll
    for (int e = 0; e < 4; e++) {
      const float w0 = bf_lo(w[i][e]), w1 = bf_hi(w[i][e]);
      acc[2 * e] += a0[i] * w0; acc[2 * e + 1] += a0[i] * w1;
      acc[8 + 2 * e] += a1[i] * w0; acc[8 + 2 * e + 1] += a1[i] * w1;
    }
  }
#pragma unroll
  for (int st2 = 0; st2 < 4; st2++) {
    const int half = 8 >> st2;
    const int o = 1 << st2;
    const bool up = (lane & o) != 0;
#pragma unroll
    for (int i = 0; i < half; i++) {
      const float keep = up ? acc[half + i] : acc[i];
      const float send = up ? acc[i] : acc[half + i];
      acc[i] = keep + __shfl_xor(send, o, 64);
    }
  }
  acc[0] += __shfl_xor(acc[0], 16, 64);
  acc[0] += __shfl_xor(acc[0], 32, 64);
  const int vi = ((lane & 1) << 3) | ((lane & 2) << 1) | ((lane & 4) >> 1) | ((lane & 8) >> 3);
  if (lane < 16) red[wv * 16 + vi] = acc[0];
  __syncthreads();
  if (tid < 16) {
    const float y = red[tid] + red[16 + tid] + red[32 + tid] + red[48 + tid];
    const float hf = __uint_as_float((unsigned)hv << 16);
    const float r = hf + (hf - mean) * rstd + y * gate;
    out[(size_t)row * D_ + cgp * 8 + (tid & 7)] = (unsigned short)(pack_bf16(r, 0.f) & 0xffffu);
  }
}

extern "C" void kernel_launch(void* const* d_in, const int* in_sizes, int n_in,
                              void* d_out, int out_size, void* d_ws, size_t ws_size,
                              hipStream_t stream) {
  const unsigned short* x = (const unsigned short*)d_in[0];
  const unsigned short* wqkv = (const unsigned short*)d_in[1];
  const unsigned short* wo = (const unsigned short*)d_in[2];
  const unsigned short* wr = (const unsigned short*)d_in[4];
  const unsigned short* wg = (const unsigned short*)d_in[5];
  const unsigned short* wu = (const unsigned short*)d_in[6];
  const unsigned short* wd = (const unsigned short*)d_in[7];
  char* ws = (char*)d_ws;
  unsigned short* out = (unsigned short*)d_out;
  (void)hipFuncSetAttribute((const void*)k1_qkv, hipFuncAttributeMaxDynamicSharedMemorySize, K1_LDS);
  (void)hipFuncSetAttribute((const void*)k2_attn, hipFuncAttributeMaxDynamicSharedMemorySize, K2_LDS);
  (void)hipFuncSetAttribute((const void*)k3_oproj, hipFuncAttributeMaxDynamicSharedMemorySize, K3_LDS);
  k1_qkv<<<256, 256, K1_LDS, stream>>>(x, wqkv, ws);
  k2_attn<<<256, 1024, K2_LDS, stream>>>(ws);
  k3_oproj<<<256, 512, K3_LDS, stream>>>(x, wo, wr, ws);
  k4_route<<<256, 256, 0, stream>>>(wr, wg, wu, ws, out);
  k5_down<<<64, 256, 0, stream>>>(wd, ws, out);
}
```

```cpp
#include <hip/hip_runtime.h>
#include <hip/hip_bf16.h>
#include <stdint.h>

typedef __attribute__((ext_vector_type(8))) short bf16x8;
typedef __attribute__((ext_vector_type(4))) short s16x4;
typedef __attribute__((ext_vector_type(2))) __bf16 bf16x2_t;
typedef __attribute__((ext_vector_type(16))) float f32x16;
typedef __attribute__((ext_vector_type(4))) float f32x4;
typedef __attribute__((ext_vector_type(2))) float f32x2;
typedef __attribute__((ext_vector_type(4))) unsigned u32x4;
typedef __attribute__((ext_vector_type(2))) unsigned u32x2;

#define DI __device__ __forceinline__
#define LDS_AS __attribute__((address_space(3)))
#define MFMA32(a, b, c) __builtin_amdgcn_mfma_f32_32x32x16_bf16((a), (b), (c), 0, 0, 0)
#define MFMA16(a, b, c) __builtin_amdgcn_mfma_f32_16x16x32_bf16((a), (b), (c), 0, 0, 0)
#define WAIT_VM(n) asm volatile("s_waitcnt vmcnt(" #n ")" ::: "memory")
#define BAR() do { asm volatile("" ::: "memory"); __builtin_amdgcn_s_barrier(); asm volatile("" ::: "memory"); } while (0)
#define RAW_BAR() do { asm volatile("s_waitcnt lgkmcnt(0)" ::: "memory"); __builtin_amdgcn_s_barrier(); asm volatile("" ::: "memory"); } while (0)

#define ST_DECL
#define ST(slot)
#define ST_FLUSH(kid)

constexpr int S_ = 2048, D_ = 512, H_ = 32, F_ = 1792;
constexpr float LN_EPS = 1e-5f;

constexpr size_t WS_Q = 0;
constexpr size_t WS_K = WS_Q + (size_t)S_ * D_ * 2;
constexpr size_t WS_V = WS_K + (size_t)S_ * D_ * 2;
constexpr size_t WS_O = WS_V + (size_t)S_ * D_ * 2;
constexpr size_t WS_H = WS_O + (size_t)S_ * D_ * 2;
constexpr size_t WS_KMAX = WS_H + (size_t)S_ * D_ * 2;
constexpr size_t WS_STAT = WS_KMAX + 32 * 64 * 4;
constexpr size_t WS_ACT = WS_STAT + 3 * S_ * 4;
constexpr size_t WS_SEL = WS_ACT + 2 * F_ * 4;

DI unsigned pack_bf16(float a, float b) {
  f32x2 v = {a, b};
  bf16x2_t r = __builtin_convertvector(v, bf16x2_t);
  return __builtin_bit_cast(unsigned, r);
}
DI float bf_lo(unsigned u) { return __uint_as_float(u << 16); }
DI float bf_hi(unsigned u) { return __uint_as_float(u & 0xffff0000u); }
DI float dot2bf(unsigned a, unsigned b, float c) {
  return __builtin_amdgcn_fdot2_f32_bf16(__builtin_bit_cast(bf16x2_t, a), __builtin_bit_cast(bf16x2_t, b), c, false);
}
DI void glds16(const void* g, char* lds_wave_base) {
  __builtin_amdgcn_global_load_lds((const __attribute__((address_space(1))) unsigned*)g,
                                   (LDS_AS unsigned*)lds_wave_base, 16, 0, 0);
}
DI void glds4(const void* g, char* lds_wave_base) {
  __builtin_amdgcn_global_load_lds((const __attribute__((address_space(1))) unsigned*)g,
                                   (LDS_AS unsigned*)lds_wave_base, 4, 0, 0);
}
DI s16x4 tr_read(const char* p) {
  return __builtin_amdgcn_ds_read_tr16_b64_v4i16((LDS_AS s16x4*)p);
}
DI bf16x8 cat8(s16x4 lo, s16x4 hi) { return __builtin_shufflevector(lo, hi, 0, 1, 2, 3, 4, 5, 6, 7); }
template <int CTRL> DI float dpp_f(float x) {
  return __builtin_bit_cast(float, __builtin_amdgcn_update_dpp(0, __builtin_bit_cast(int, x), CTRL, 0xF, 0xF, true));
}
DI float add_x32(float v) {
  auto r = __builtin_amdgcn_permlane32_swap(__float_as_uint(v), __float_as_uint(v), false, false);
  return __uint_as_float(r[0]) + __uint_as_float(r[1]);
}
DI float max_x32(float v) {
  auto r = __builtin_amdgcn_permlane32_swap(__float_as_uint(v), __float_as_uint(v), false, false);
  return fmaxf(__uint_as_float(r[0]), __uint_as_float(r[1]));
}
DI float add_x16(float v) {
  auto r = __builtin_amdgcn_permlane16_swap(__float_as_uint(v), __float_as_uint(v), false, false);
  return __uint_as_float(r[0]) + __uint_as_float(r[1]);
}
DI float max_x16(float v) {
  auto r = __builtin_amdgcn_permlane16_swap(__float_as_uint(v), __float_as_uint(v), false, false);
  return fmaxf(__uint_as_float(r[0]), __uint_as_float(r[1]));
}
DI float add_8(float v) { v += dpp_f<0xB1>(v); v += dpp_f<0x4E>(v); v += dpp_f<0x141>(v); return v; }
DI float add_16(float v) { v = add_8(v); v += dpp_f<0x140>(v); return v; }
DI float add_64(float v) { return add_x32(add_x16(add_16(v))); }
DI float max_16(float v) {
  v = fmaxf(v, dpp_f<0xB1>(v)); v = fmaxf(v, dpp_f<0x4E>(v)); v = fmaxf(v, dpp_f<0x141>(v)); v = fmaxf(v, dpp_f<0x140>(v));
  return v;
}
DI float max_32(float v) { return max_x16(max_16(v)); }
DI float max_64(float v) { return max_x32(max_32(v)); }

constexpr int K1_XB = 128 * 64 * 2;
constexpr int K1_WB = 64 * 96 * 2;
constexpr int K1_ST = K1_XB + K1_WB;
constexpr int K1_NB = 5;
constexpr int K1_AUX = K1_NB * K1_ST;
constexpr int K1_LDS = K1_AUX + 4 * 96 * 4 + 2 * 128 * 4;

__global__ __launch_bounds__(1024) void k1_qkv(const unsigned short* __restrict__ x, const unsigned short* __restrict__ w,
                                               char* __restrict__ ws) {
  extern __shared__ __attribute__((aligned(16))) char smem[];
  const int tid = threadIdx.x, lane = tid & 63;
  const int wv = __builtin_amdgcn_readfirstlane(tid >> 6);
  const int b = blockIdx.x, xcd = b & 7, bi = b >> 3;
  const int hp = 2 * xcd + (bi & 1), tb = bi >> 1;
  const int m0 = tb * 128;
  ST_DECL ST(0);

  const unsigned short* xsrc;
  {
    const int row = 8 * wv + (lane >> 3);
    const int c = (lane & 7) ^ ((row >> 1) & 7);
    xsrc = x + (size_t)(m0 + row) * D_ + c * 8;
  }
  const unsigned short* wsrc;
  {
    const int l48 = lane < 48 ? lane : 0;
    const int kr = l48 / 12, s = l48 - kr * 12;
    wsrc = w + (size_t)(4 * wv + kr) * (3 * D_) + (s >> 2) * D_ + hp * 32 + (s & 3) * 8;
  }
  auto stage = [&](int c) {
    char* base = smem + (c % K1_NB) * K1_ST;
    glds16(xsrc + c * 64, base + wv * 1024);
    if (lane < 48) glds16(wsrc + (size_t)c * 64 * (3 * D_), base + K1_XB + wv * 768);
  };
#pragma unroll
  for (int c = 0; c < K1_NB; c++) { stage(c); if (c < K1_NB - 1) BAR(); }

  if (b < 6) ((float*)(ws + WS_STAT))[b * 1024 + tid] = 0.f;

  const int r = lane & 31, h = lane >> 5;
  const int m = wv & 3, role = wv >> 2;
  const int xsw = (r >> 1) & 7;
  const int xrow_off = (32 * m + r) * 128;
  const int blk = (lane >> 4) & 1, q = (lane & 15) >> 2, p = lane & 3;
  const int woff = K1_XB + (8 * h + q) * 192 + blk * 32 + p * 8;

  f32x16 aq, ak, av;
#pragma unroll
  for (int i = 0; i < 16; i++) { aq[i] = 0.f; ak[i] = 0.f; av[i] = 0.f; }
  float s0 = 0.f, s1 = 0.f, q0 = 0.f, q1 = 0.f;
  const unsigned ONES = 0x3f803f80u;
  bf16x8 xf, wq, wk, wvv;
  auto ldx = [&](int c, int ks) { return *(const bf16x8*)(smem + (c % K1_NB) * K1_ST + xrow_off + (((2 * ks + h) ^ xsw) << 4)); };
  auto ldw = [&](int c, int ks, int sg) {
    const char* pw = smem + (c % K1_NB) * K1_ST + woff + ks * 16 * 192 + sg * 64;
    return cat8(tr_read(pw), tr_read(pw + 4 * 192));
  };

  WAIT_VM(8);
  RAW_BAR();
  ST(1);
  if (role == 0) { xf = ldx(0, 0); wq = ldw(0, 0, 0); wk = ldw(0, 0, 1); wvv = ldw(0, 0, 2); }
#pragma unroll
  for (int c = 0; c < 8; c++) {
    if (c == 0) WAIT_VM(6);
    else if (c <= 4) WAIT_VM(4);
    else if (c == 5) WAIT_VM(2);
    else WAIT_VM(0);
    if (c < 7) BAR();
    if (c == 4) ST(2);
    if (c >= 1 && c <= 3) stage(c + 4);
    if (role == 0) {
#pragma unroll
      for (int ks = 0; ks < 4; ks++) {
        const int nc = ks == 3 ? c + 1 : c, nk = (ks + 1) & 3;
        bf16x8 nx, nq, nkk, nv;
        if (nc < 8) { nx = ldx(nc, nk); nq = ldw(nc, nk, 0); nkk = ldw(nc, nk, 1); nv = ldw(nc, nk, 2); }
        __builtin_amdgcn_sched_barrier(0);
        aq = MFMA32(wq, xf, aq);
        ak = MFMA32(wk, xf, ak);
        av = MFMA32(xf, wvv, av);
        if (ks == m) {
          u32x4 a0 = __builtin_bit_cast(u32x4, wq), a1 = __builtin_bit_cast(u32x4, wk), a2 = __builtin_bit_cast(u32x4, wvv);
#pragma unroll
          for (int e = 0; e < 4; e++) { s0 = dot2bf(a0[e], ONES, s0); s1 = dot2bf(a1[e], ONES, s1); q0 = dot2bf(a2[e], ONES, q0); }
        }
        __builtin_amdgcn_sched_barrier(0);
        if (nc < 8) { xf = nx; wq = nq; wk = nkk; wvv = nv; }
      }
    } else if (role == 1) {
#pragma unroll
      for (int ks = 0; ks < 4; ks++) {
        u32x4 xu = __builtin_bit_cast(u32x4, ldx(c, ks));
        s0 = dot2bf(xu[0], ONES, s0); s1 = dot2bf(xu[1], ONES, s1);
        s0 = dot2bf(xu[2], ONES, s0); s1 = dot2bf(xu[3], ONES, s1);
        q0 = dot2bf(xu[0], xu[0], q0); q1 = dot2bf(xu[1], xu[1], q1);
        q0 = dot2bf(xu[2], xu[2], q0); q1 = dot2bf(xu[3], xu[3], q1);
      }
      asm volatile("s_waitcnt lgkmcnt(0)" ::: "memory");
    }
  }
  ST(3);
  float* csl = (float*)(smem + K1_AUX);
  float* str = csl + 4 * 96;
  float* stm = str + 128;
  if (role == 0) {
    const float c0 = add_x32(s0), c1 = add_x32(s1), c2 = add_x32(q0);
    if (h == 0) { csl[m * 96 + r] = c0; csl[m * 96 + 32 + r] = c1; csl[m * 96 + 64 + r] = c2; }
  } else if (role == 1) {
    const float sum = add_x32(s0 + s1), sq = add_x32(q0 + q1);
    const float mean = sum * (1.f / D_);
    const float var = fmaxf(sq * (1.f / D_) - mean * mean, 0.f);
    if (h == 0) { str[32 * m + r] = rsqrtf(var + LN_EPS); stm[32 * m + r] = mean; }
  }
  __syncthreads();
  if (role == 0) {
    const int T = tb * 4 + m;
    {
      const float cv = csl[64 + r] + csl[96 + 64 + r] + csl[192 + 64 + r] + csl[288 + 64 + r];
      float vv[16];
#pragma unroll
      for (int g = 0; g < 4; g++) {
        const f32x4 a = *(const f32x4*)(str + 32 * m + 8 * g + 4 * h);
        const f32x4 bb = *(const f32x4*)(stm + 32 * m + 8 * g + 4 * h);
#pragma unroll
        for (int e = 0; e < 4; e++) vv[4 * g + e] = (av[4 * g + e] - bb[e] * cv) * a[e];
      }
      const int head = 2 * hp + (r >> 4), d = r & 15;
#pragma unroll
      for (int s = 0; s < 2; s++) {
        u32x4 pv;
#pragma unroll
        for (int e = 0; e < 4; e++) pv[e] = pack_bf16(vv[8 * s + 2 * e], vv[8 * s + 2 * e + 1]);
        *(u32x4*)(ws + WS_V + (size_t)(head * 64 + T) * 1024 + s * 512 + h * 256 + d * 16) = pv;
      }
    }
    const float mean = stm[32 * m + r];
    const float rs = str[32 * m + r];
    const float rq = rs * (0.25f * 1.4426950408889634f);
    float vq[16], vk[16];
#pragma unroll
    for (int g = 0; g < 4; g++) {
      f32x4 cq = {0.f, 0.f, 0.f, 0.f}, ck = {0.f, 0.f, 0.f, 0.f};
#pragma unroll
      for (int ww = 0; ww < 4; ww++) {
        cq += *(const f32x4*)(csl + ww * 96 + 8 * g + 4 * h);
        ck += *(const f32x4*)(csl + ww * 96 + 32 + 8 * g + 4 * h);
      }
#pragma unroll
      for (int e = 0; e < 4; e++) {
        vq[4 * g + e] = (aq[4 * g + e] - mean * cq[e]) * rq;
        vk[4 * g + e] = (ak[4 * g + e] - mean * ck[e]) * rs;
      }
    }
#pragma unroll
    for (int hh = 0; hh < 2; hh++) {
      const int head = 2 * hp + hh;
      u32x4 pq, pk;
      float n2 = 0.f;
#pragma unroll
      for (int e = 0; e < 4; e++) {
        pq[e] = pack_bf16(vq[8 * hh + 2 * e], vq[8 * hh + 2 * e + 1]);
        pk[e] = pack_bf16(vk[8 * hh + 2 * e], vk[8 * hh + 2 * e + 1]);
        n2 = dot2bf(pk[e], pk[e], n2);
      }
      const size_t off = (size_t)(head * 64 + T) * 1024 + lane * 16;
      *(u32x4*)(ws + WS_K + off) = pk;
      *(u32x4*)(ws + WS_Q + off) = pq;
      n2 = max_32(add_x32(n2));
      if (lane == 0) ((float*)(ws + WS_KMAX))[head * 64 + T] = n2;
    }
  }
  ST(4); ST_FLUSH(0);
}

constexpr int K2_RING = 0;
constexpr int K2_ONES = 131072;
constexpr int K2_Q = K2_ONES + 2048;
constexpr int K2_KM = K2_Q + 8192;
constexpr int K2_SC = K2_KM + 512;
constexpr int K2_LDS = K2_SC + 16384;

DI void exp_pack(const f32x16& s, bf16x8& p0, bf16x8& p1) {
  u32x4 a, c;
#pragma unroll
  for (int e = 0; e < 4; e++) {
    a[e] = pack_bf16(__builtin_amdgcn_exp2f(s[2 * e]), __builtin_amdgcn_exp2f(s[2 * e + 1]));
    c[e] = pack_bf16(__builtin_amdgcn_exp2f(s[8 + 2 * e]), __builtin_amdgcn_exp2f(s[8 + 2 * e + 1]));
  }
  p0 = __builtin_bit_cast(bf16x8, a);
  p1 = __builtin_bit_cast(bf16x8, c);
}
#define LD8(p) (*(const bf16x8*)(p))
#define SB() __builtin_amdgcn_sched_barrier(0)
#define EX2(x) __builtin_amdgcn_exp2f(x)

template <int NS>
DI void attn_seg(const char* ka, const char* va, int vmask, const bf16x8 qf, const f32x16& cinit, f32x16& o) {
  constexpr int NT = 2 * NS;
  bf16x8 kn = LD8(ka + 1024);
  f32x16 s = MFMA32(LD8(ka), qf, cinit);
  bf16x8 v0 = LD8(va), v1 = LD8(va + 512);
  bf16x8 vprev, pprev;
#pragma unroll
  for (int j = 0; j < NT; j++) {
    if ((j & 1) == 0) {
      if (NS - 1 - (j >> 1) == 3) __builtin_amdgcn_s_setprio(3);
      else if (NS - 1 - (j >> 1) == 2) __builtin_amdgcn_s_setprio(2);
      else if (NS - 1 - (j >> 1) == 1) __builtin_amdgcn_s_setprio(1);
      else __builtin_amdgcn_s_setprio(0);
    }
    const char* vp = va + (vmask & (((j + 1) >> 1) * 16384)) + ((j + 1) & 1) * 1024;
    const char* kp = ka + ((j + 2) >> 1) * 16384 + ((j + 2) & 1) * 1024;
    float e0[8], e1[8];
    u32x4 pa, pc;
    f32x16 sNext;
    bf16x8 w0, w1, k2;
    SB();
#pragma unroll
    for (int e = 0; e < 4; e++) e0[e] = EX2(s[e]);
    SB();
    if (j > 0) o = MFMA32(vprev, pprev, o);
    SB();
#pragma unroll
    for (int e = 4; e < 8; e++) e0[e] = EX2(s[e]);
#pragma unroll
    for (int e = 0; e < 4; e++) pa[e] = pack_bf16(e0[2 * e], e0[2 * e + 1]);
    SB();
    if (j + 1 < NT) { sNext = MFMA32(kn, qf, cinit); w0 = LD8(vp); w1 = LD8(vp + 512); }
    if (j + 2 < NT) k2 = LD8(kp);
    SB();
#pragma unroll
    for (int e = 0; e < 5; e++) e1[e] = EX2(s[8 + e]);
    SB();
    o = MFMA32(v0, __builtin_bit_cast(bf16x8, pa), o);
    SB();
#pragma unroll
    for (int e = 5; e < 8; e++) e1[e] = EX2(s[8 + e]);
#pragma unroll
    for (int e = 0; e < 4; e++) pc[e] = pack_bf16(e1[2 * e], e1[2 * e + 1]);
    pprev = __builtin_bit_cast(bf16x8, pc); vprev = v1;
    if (j + 1 < NT) { s = sNext; v0 = w0; v1 = w1; }
    if (j + 2 < NT) kn = k2;
  }
  SB();
  o = MFMA32(vprev, pprev, o);
}

__global__ __launch_bounds__(1024) void k2_attn(char* __restrict__ ws) {
  extern __shared__ __attribute__((aligned(16))) char smem[];
  const int tid = threadIdx.x, lane = tid & 63;
  const int wv = __builtin_amdgcn_readfirstlane(tid >> 6);
  const int b = blockIdx.x, xcd = b & 7, bi = b >> 3;
  const int hp = 2 * xcd + (bi & 1), tb = bi >> 1;
  const int a = wv >> 3, kh = (wv >> 2) & 1, qt = wv & 3;
  const int head = 2 * hp + a;
  ST_DECL ST(0);

  if (wv < 8) glds16(ws + WS_Q + (size_t)((2 * hp + (wv >> 2)) * 64 + tb * 4 + (wv & 3)) * 1024 + lane * 16, smem + K2_Q + wv * 1024);
  else if (wv < 10) glds4(ws + WS_KMAX + (size_t)((2 * hp + (wv - 8)) * 64 + lane) * 4, smem + K2_KM + (wv - 8) * 256);
  const char* gsrc = ws + (((wv >> 2) & 1) ? WS_V : WS_K) + (size_t)(head * 64 + (wv & 3)) * 1024 + lane * 16;
  auto fill = [&](int s) {
    glds16(gsrc + (size_t)((tb + s) & 15) * 4096, smem + K2_RING + (s & 7) * 16384 + wv * 1024);
  };
#pragma unroll
  for (int s = 0; s < 8; s++) { fill(s); if (s == 0 || s == 3) BAR(); }
  if (tid < 128) {
    const unsigned o = ((tid & 15) == 0 || (tid & 15) == 4) ? 0x3f803f80u : 0u;
    *(u32x4*)(smem + K2_ONES + tid * 16) = (u32x4){o, o, o, o};
  }
  WAIT_VM(7);
  RAW_BAR();
  ST(1);

  const bf16x8 qf = LD8(smem + K2_Q + (a * 4 + qt) * 1024 + lane * 16);
  float mrow;
  {
    const float km = max_64(*(const float*)(smem + K2_KM + a * 256 + lane * 4));
    u32x4 qu = __builtin_bit_cast(u32x4, qf);
    float q2 = dot2bf(qu[0], qu[0], 0.f);
    q2 = dot2bf(qu[1], qu[1], q2); q2 = dot2bf(qu[2], qu[2], q2); q2 = dot2bf(qu[3], qu[3], q2);
    q2 = add_x32(q2);
    mrow = __builtin_amdgcn_sqrtf(q2 * km) * 1.001f;
  }
  const int kbase = K2_RING + a * 8192 + kh * 2048 + lane * 16;
  const int vl = (lane >> 5) * 256 + (lane & 15) * 16;
  const bool ones_lane = (lane & 16) != 0;
  const int vbase = ones_lane ? (K2_ONES + vl) : (K2_RING + a * 8192 + 4096 + kh * 2048 + vl);
  const int vmask = ones_lane ? 0 : -1;

  f32x16 cinit, o;
#pragma unroll
  for (int i = 0; i < 16; i++) { cinit[i] = -mrow; o[i] = 0.f; }
  ST(2);
  attn_seg<1>(smem + kbase, smem + vbase, vmask, qf, cinit, o);
  WAIT_VM(4);
  RAW_BAR();
  attn_seg<3>(smem + kbase + 16384, smem + vbase + (vmask & 16384), vmask, qf, cinit, o);
#pragma unroll 1
  for (int u = 1; u < 4; u++) {
    WAIT_VM(0);
    RAW_BAR();
    if (u < 3) { fill(4 * u + 4); fill(4 * u + 5); fill(4 * u + 6); fill(4 * u + 7); }
    const int so = (u & 1) * 65536;
    attn_seg<4>(smem + kbase + so, smem + vbase + (vmask & so), vmask, qf, cinit, o);
  }
  __builtin_amdgcn_s_setprio(0);
  ST(3);
  float* lx = (float*)(smem + K2_Q);
  float* mx = (float*)(smem + K2_Q + 4096);
  float* sc = (float*)(smem + K2_SC) + (a * 4 + qt) * 512;
  const int pw = wv ^ 4;
  lx[wv * 64 + lane] = o[8];
  if (kh == 1) {
#pragma unroll
    for (int i = 0; i < 8; i++) sc[i * 64 + lane] = o[i];
  }
  __syncthreads();
  float ltot = o[8] + lx[pw * 64 + lane];
  const bool bad = !(ltot >= 9.094947e-13f);
  if (__any(bad)) {
    const char* gk = ws + WS_K + (size_t)(head * 64 + kh * 32) * 1024 + lane * 16;
    const char* gv = ws + WS_V + (size_t)(head * 64 + kh * 32) * 1024 + vl;
    float mt = -INFINITY;
    f32x16 zero;
#pragma unroll
    for (int i = 0; i < 16; i++) zero[i] = 0.f;
#pragma unroll 1
    for (int t = 0; t < 32; t++) {
      f32x16 sv = MFMA32(LD8(gk + t * 1024), qf, zero);
#pragma unroll
      for (int i = 0; i < 16; i++) mt = fmaxf(mt, sv[i]);
    }
    mt = max_x32(mt);
    mx[wv * 64 + lane] = mt;
    __syncthreads();
    mt = fmaxf(mt, mx[pw * 64 + lane]);
    const bf16x8 cst = LD8(smem + K2_ONES + vl);
#pragma unroll
    for (int i = 0; i < 16; i++) { cinit[i] = -mt; o[i] = 0.f; }
#pragma unroll 1
    for (int t = 0; t < 32; t++) {
      f32x16 sv = MFMA32(LD8(gk + t * 1024), qf, cinit);
      bf16x8 w0 = LD8(gv + t * 1024), w1 = LD8(gv + t * 1024 + 512);
      if (ones_lane) { w0 = cst; w1 = cst; }
      bf16x8 p0, p1;
      exp_pack(sv, p0, p1);
      o = MFMA32(w0, p0, o);
      o = MFMA32(w1, p1, o);
    }
    lx[wv * 64 + lane] = o[8];
    if (kh == 1) {
#pragma unroll
      for (int i = 0; i < 8; i++) sc[i * 64 + lane] = o[i];
    }
    __syncthreads();
    ltot = o[8] + lx[pw * 64 + lane];
  }
  if (kh == 0) {
    const float inv = 1.f / ltot;
    u32x4 po;
#pragma unroll
    for (int e = 0; e < 4; e++) {
      const float x0 = (o[2 * e] + sc[(2 * e) * 64 + lane]) * inv;
      const float x1 = (o[2 * e + 1] + sc[(2 * e + 1) * 64 + lane]) * inv;
      po[e] = pack_bf16(x0, x1);
    }
    *(u32x4*)(ws + WS_O + (size_t)((tb * 4 + qt) * 32 + head) * 1024 + lane * 16) = po;
  }
  ST(4); ST_FLUSH(1);
}

constexpr int K3_W = 0;
constexpr int K3_RED = 65536;
constexpr int K3_LDS = K3_RED + 4 * 64 * 272;

__global__ __launch_bounds__(512) void k3_oproj(const unsigned short* __restrict__ x, const unsigned short* __restrict__ wo,
                                                const unsigned short* __restrict__ wr, char* __restrict__ ws) {
  extern __shared__ __attribute__((aligned(16))) char smem[];
  const int tid = threadIdx.x, lane = tid & 63;
  const int wv = __builtin_amdgcn_readfirstlane(tid >> 6);
  const int b = blockIdx.x, xcd = b & 7, bi = b >> 3;
  const int mt = (xcd >> 1) * 8 + (bi & 7), nt = (xcd & 1) * 4 + (bi >> 3);
  const int m = wv & 1, kq = wv >> 1;

  ST_DECL ST(0);
  {
    const int rr = lane >> 3;
    const int c = (lane & 7) ^ (((rr >> 1) & 1) << 2);
    const unsigned short* src = wo + (size_t)(64 * wv + rr) * D_ + nt * 64 + c * 8;
#pragma unroll
    for (int i = 0; i < 8; i++) glds16(src + (size_t)i * 8 * D_, smem + K3_W + (8 * wv + i) * 1024);
  }
  bf16x8 of[8];
  {
    const char* src = ws + WS_O + (size_t)((mt * 2 + m) * 32 + kq * 8) * 1024 + lane * 16;
#pragma unroll
    for (int i = 0; i < 8; i++) of[i] = *(const bf16x8*)(src + i * 1024);
  }
  const int tok = tid >> 3, cg = tid & 7;
  const u32x4 xr = *(const u32x4*)(x + (size_t)(mt * 64 + tok) * D_ + nt * 64 + cg * 8);
  const u32x4 wrv = *(const u32x4*)(wr + nt * 64 + cg * 8);

  WAIT_VM(0);
  RAW_BAR();
  ST(1);

  const int h = lane >> 5, blk = (lane >> 4) & 1, q = (lane & 15) >> 2, p = lane & 3;
  f32x16 acc[2];
#pragma unroll
  for (int i = 0; i < 16; i++) { acc[0][i] = 0.f; acc[1][i] = 0.f; }
  const int rsw = ((q >> 1) & 1) << 2;
#pragma unroll
  for (int i = 0; i < 8; i++) {
    const char* rowp = smem + K3_W + (kq * 128 + i * 16 + 4 * h + q) * 128;
#pragma unroll
    for (int n = 0; n < 2; n++) {
      const int chunk = (n * 4 + blk * 2 + (p >> 1)) ^ rsw;
      const char* pw = rowp + chunk * 16 + (p & 1) * 8;
      bf16x8 wf = cat8(tr_read(pw), tr_read(pw + 8 * 128));
      acc[n] = MFMA32(wf, of[i], acc[n]);
    }
  }
  ST(2);
  float* red = (float*)(smem + K3_RED);
  {
    float* dst = red + (kq * 64 + m * 32 + (lane & 31)) * 68 + 4 * h;
#pragma unroll
    for (int n = 0; n < 2; n++)
#pragma unroll
      for (int g = 0; g < 4; g++)
        *(f32x4*)(dst + n * 32 + 8 * g) = (f32x4){acc[n][4 * g], acc[n][4 * g + 1], acc[n][4 * g + 2], acc[n][4 * g + 3]};
  }
  __syncthreads();
  f32x4 a0 = {0.f, 0.f, 0.f, 0.f}, a1 = {0.f, 0.f, 0.f, 0.f};
#pragma unroll
  for (int k = 0; k < 4; k++) {
    const float* src = red + (k * 64 + tok) * 68 + cg * 8;
    a0 += *(const f32x4*)src;
    a1 += *(const f32x4*)(src + 4);
  }
  u32x4 hp;
  hp[0] = pack_bf16(a0[0] + bf_lo(xr[0]), a0[1] + bf_hi(xr[0]));
  hp[1] = pack_bf16(a0[2] + bf_lo(xr[1]), a0[3] + bf_hi(xr[1]));
  hp[2] = pack_bf16(a1[0] + bf_lo(xr[2]), a1[1] + bf_hi(xr[2]));
  hp[3] = pack_bf16(a1[2] + bf_lo(xr[3]), a1[3] + bf_hi(xr[3]));
  *(u32x4*)(ws + WS_H + ((size_t)(mt * 64 + tok) * D_ + nt * 64 + cg * 8) * 2) = hp;
  const unsigned ONES = 0x3f803f80u;
  float s1 = 0.f, s2 = 0.f, s3 = 0.f;
#pragma unroll
  for (int e = 0; e < 4; e++) {
    s1 = dot2bf(hp[e], ONES, s1);
    s2 = dot2bf(hp[e], hp[e], s2);
    s3 = dot2bf(hp[e], wrv[e], s3);
  }
  s1 = add_8(s1); s2 = add_8(s2); s3 = add_8(s3);
  if (cg == 0) {
    float* st = (float*)(ws + WS_STAT);
    const int row = mt * 64 + tok;
    atomicAdd(st + row, s1);
    atomicAdd(st + S_ + row, s2);
    atomicAdd(st + 2 * S_ + row, s3);
  }
  ST(3); ST_FLUSH(2);
}

constexpr int K4_W = 0;
constexpr int K4_XS = 16384;
constexpr int K4_TP = K4_XS + 2048;
constexpr int K4_LDS = K4_TP + 128;

DI void wave_top2(float v1, int i1, float v2, int i2, float& M1, int& I1, float& M2, int& I2) {
  M1 = max_64(v1);
  unsigned long long bm = __ballot(v1 == M1);
  int l1 = bm ? __builtin_ctzll(bm) : 0;
  I1 = __builtin_amdgcn_readlane(i1, l1);
  const bool me = (int)(threadIdx.x & 63) == l1;
  const float c = me ? v2 : v1;
  const int ci = me ? i2 : i1;
  M2 = max_64(c);
  bm = __ballot(c == M2);
  int l2 = bm ? __builtin_ctzll(bm) : 0;
  I2 = __builtin_amdgcn_readlane(ci, l2);
}

__global__ __launch_bounds__(512) void k4_route(const unsigned short* __restrict__ wr, const unsigned short* __restrict__ wg,
                                                const unsigned short* __restrict__ wu, char* __restrict__ ws,
                                                unsigned short* __restrict__ out) {
  extern __shared__ __attribute__((aligned(16))) char smem[];
  const int tid = threadIdx.x, lane = tid & 63;
  const int wv = __builtin_amdgcn_readfirstlane(tid >> 6);
  const int b = blockIdx.x;
  const int xcd = b & 7, bi = b >> 3;
  const int cgp = xcd * 28 + bi;
  const bool ffn = bi < 28;
  ST_DECL ST(0);
  const float* st = (const float*)(ws + WS_STAT);
  const f32x4 a = *(const f32x4*)(st + tid * 4);
  const f32x4 c = *(const f32x4*)(st + S_ + tid * 4);
  const f32x4 d = *(const f32x4*)(st + 2 * S_ + tid * 4);
  const u32x4 wv4 = *(const u32x4*)(wr + lane * 8);
  const int orow = b * 8 + wv;
  const unsigned short* hsrc = (const unsigned short*)(ws + WS_H);
  const u32x4 h0 = *(const u32x4*)(hsrc + (size_t)orow * D_ + lane * 8);
  const float os1 = st[orow], os2 = st[S_ + orow];
  if (ffn) {
#pragma unroll
    for (int i = 0; i < 2; i++) {
      const int slot = (wv * 2 + i) * 64 + lane;
      const unsigned short* src = ((slot & 1) ? wu : wg) + (size_t)(slot >> 1) * F_ + cgp * 8;
      glds16(src, smem + K4_W + (wv * 2 + i) * 1024);
    }
  }
  asm volatile("" ::: "memory");
  float wsum;
  {
    const unsigned ONES = 0x3f803f80u;
    wsum = dot2bf(wv4[0], ONES, 0.f); wsum = dot2bf(wv4[1], ONES, wsum);
    wsum = dot2bf(wv4[2], ONES, wsum); wsum = dot2bf(wv4[3], ONES, wsum);
    wsum = add_64(wsum);
  }
  ST(1);
  float v1 = -INFINITY, v2 = -INFINITY;
  int i1 = tid * 4, i2 = tid * 4 + 1;
#pragma unroll
  for (int i = 0; i < 4; i++) {
    const float mean = a[i] * (1.f / D_);
    const float var = fmaxf(c[i] * (1.f / D_) - mean * mean, 0.f);
    const float rr = rsqrtf(var + LN_EPS) * (d[i] - mean * wsum);
    const int idx = tid * 4 + i;
    const bool g1 = rr > v1, g2 = rr > v2;
    v2 = g1 ? v1 : (g2 ? rr : v2); i2 = g1 ? i1 : (g2 ? idx : i2);
    v1 = g1 ? rr : v1; i1 = g1 ? idx : i1;
  }
  float M1, M2; int I1, I2;
  wave_top2(v1, i1, v2, i2, M1, I1, M2, I2);
  float* tp = (float*)(smem + K4_TP);
  if (lane == 0) *(f32x4*)(tp + wv * 4) = (f32x4){M1, __int_as_float(I1), M2, __int_as_float(I2)};
  ST(2);
  {
    const float mean = os1 * (1.f / D_);
    const float rstd = rsqrtf(fmaxf(os2 * (1.f / D_) - mean * mean, 0.f) + LN_EPS);
    const float sc = 1.f + rstd, of = -mean * rstd;
    u32x4 o0;
#pragma unroll
    for (int e = 0; e < 4; e++) o0[e] = pack_bf16(bf_lo(h0[e]) * sc + of, bf_hi(h0[e]) * sc + of);
    *(u32x4*)(out + (size_t)orow * D_ + lane * 8) = o0;
  }
  if (!ffn) return;
  RAW_BAR();
  if (wv >= 2) { WAIT_VM(0); RAW_BAR(); return; }
  {
    const f32x4 t = *(const f32x4*)(tp + (lane & 7) * 4);
    wave_top2(t[0], __float_as_int(t[1]), t[2], __float_as_int(t[3]), M1, I1, M2, I2);
    const float c1 = (__float_as_int(t[1]) == I1) ? t[2] : t[0];
    const int ci = (__float_as_int(t[1]) == I1) ? __float_as_int(t[3]) : __float_as_int(t[1]);
    M2 = max_64(c1);
    unsigned long long bm = __ballot(c1 == M2);
    I2 = __builtin_amdgcn_readlane(ci, bm ? __builtin_ctzll(bm) : 0);
  }
  I1 &= (S_ - 1); I2 &= (S_ - 1);
  ST(3);
  {
    const int row = wv ? I2 : I1;
    const u32x4 hv = *(const u32x4*)(hsrc + (size_t)row * D_ + lane * 8);
    const float mean = st[row] * (1.f / D_);
    const float rstd = rsqrtf(fmaxf(st[S_ + row] * (1.f / D_) - mean * mean, 0.f) + LN_EPS);
    u32x4 xn;
#pragma unroll
    for (int e = 0; e < 4; e++) xn[e] = pack_bf16((bf_lo(hv[e]) - mean) * rstd, (bf_hi(hv[e]) - mean) * rstd);
    *(u32x4*)(smem + K4_XS + wv * 1024 + lane * 16) = xn;
    if (b == 0 && lane == 0) {
      float* sel = (float*)(ws + WS_SEL);
      const float rv = wv ? M2 : M1;
      ((int*)sel)[wv] = row;
      sel[2 + wv] = 1.f / (1.f + __expf(-rv));
      sel[4 + 2 * wv] = mean;
      sel[5 + 2 * wv] = rstd;
    }
  }
  WAIT_VM(0);
  RAW_BAR();
  ST(4);
  if (wv == 0) {
    const int g = lane >> 4, q = (lane & 15) >> 2, p = lane & 3;
    const char* xa = smem + K4_XS + (lane & 1) * 1024 + g * 16;
    const char* wa = smem + K4_W + (8 * g + q) * 32 + p * 8;
    f32x4 c0 = {0.f, 0.f, 0.f, 0.f}, c1 = {0.f, 0.f, 0.f, 0.f};
#pragma unroll
    for (int s = 0; s < 16; s += 2) {
      c0 = MFMA16(LD8(xa + s * 64), cat8(tr_read(wa + s * 1024), tr_read(wa + s * 1024 + 128)), c0);
      c1 = MFMA16(LD8(xa + s * 64 + 64), cat8(tr_read(wa + s * 1024 + 1024), tr_read(wa + s * 1024 + 1152)), c1);
    }
    const float g0 = c0[0] + c1[0], g1 = c0[1] + c1[1];
    const float u0 = dpp_f<0x128>(g0), u1 = dpp_f<0x128>(g1);
    if (lane < 8) {
      const unsigned gp = pack_bf16(g0, g1), up = pack_bf16(u0, u1);
      const float ga = bf_lo(gp), gb = bf_hi(gp);
      const unsigned sp = pack_bf16(ga / (1.f + __expf(-ga)), gb / (1.f + __expf(-gb)));
      const unsigned ap = pack_bf16(bf_lo(sp) * bf_lo(up), bf_hi(sp) * bf_hi(up));
      unsigned short* act = (unsigned short*)(ws + WS_ACT);
      act[cgp * 8 + lane] = (unsigned short)(ap & 0xffffu);
      act[F_ + cgp * 8 + lane] = (unsigned short)(ap >> 16);
    }
  }
  ST(5); ST_FLUSH(3);
}

constexpr int K5_W = 0;
constexpr int K5_A = 28672;
constexpr int K5_R = K5_A + 7168;
constexpr int K5_LDS = K5_R + 256;

__global__ __launch_bounds__(1024) void k5_down(const unsigned short* __restrict__ wd, char* __restrict__ ws,
                                                unsigned short* __restrict__ out) {
  extern __shared__ __attribute__((aligned(16))) char smem[];
  const int tid = threadIdx.x, lane = tid & 63;
  const int wv = __builtin_amdgcn_readfirstlane(tid >> 6);
  const int b = blockIdx.x;
  const int cgp = (b & 7) * 8 + (b >> 3);
  ST_DECL ST(0);
  glds16(wd + (size_t)(wv * 64 + lane) * D_ + cgp * 8, smem + K5_W + wv * 1024);
  if (wv < 12) glds16(wd + (size_t)((16 + wv) * 64 + lane) * D_ + cgp * 8, smem + K5_W + (16 + wv) * 1024);
  if (wv >= 9) glds16(ws + WS_ACT + (wv - 9) * 1024 + lane * 16, smem + K5_A + (wv - 9) * 1024);
  const float* sel = (const float*)(ws + WS_SEL);
  const int tk = (lane >> 3) & 1;
  int row = 0; float gate = 0.f, mean = 0.f, rstd = 0.f, hf = 0.f;
  if (wv == 0) {
    row = ((const int*)sel)[tk] & (S_ - 1);
    gate = sel[2 + tk]; mean = sel[4 + 2 * tk]; rstd = sel[5 + 2 * tk];
    hf = __uint_as_float((unsigned)((const unsigned short*)(ws + WS_H))[(size_t)row * D_ + cgp * 8 + (lane & 7)] << 16);
  }
  WAIT_VM(0);
  RAW_BAR();
  ST(1);
  float* red = (float*)(smem + K5_R);
  if (wv < 4) {
    const int g = lane >> 4, q = (lane & 15) >> 2, p = lane & 3;
    const char* xa = smem + K5_A + (lane & 1) * 3584 + ((lane >> 1) & 1) * 1792 + g * 16 + wv * 7 * 64;
    const char* wa = smem + K5_W + ((p >> 1) * 896 + wv * 7 * 32 + 8 * g + q) * 16 + (p & 1) * 8;
    f32x4 c0 = {0.f, 0.f, 0.f, 0.f};
#pragma unroll
    for (int s = 0; s < 7; s++)
      c0 = MFMA16(LD8(xa + s * 64), cat8(tr_read(wa + s * 512), tr_read(wa + s * 512 + 64)), c0);
    const float y0 = c0[0] + dpp_f<0x128>(c0[2]), y1 = c0[1] + dpp_f<0x128>(c0[3]);
    if (lane < 8) { red[wv * 16 + lane] = y0; red[wv * 16 + 8 + lane] = y1; }
  }
  RAW_BAR();
  ST(2);
  if (tid < 16) {
    const float y = red[tid] + red[16 + tid] + red[32 + tid] + red[48 + tid];
    const float r = hf + (hf - mean) * rstd + y * gate;
    out[(size_t)row * D_ + cgp * 8 + (tid & 7)] = (unsigned short)(pack_bf16(r, 0.f) & 0xffffu);
  }
  ST(3); ST_FLUSH(4);
}

extern "C" void kernel_launch(void* const* d_in, const int* in_sizes, int n_in,
                              void* d_out, int out_size, void* d_ws, size_t ws_size,
                              hipStream_t stream) {
  const unsigned short* x = (const unsigned short*)d_in[0];
  const unsigned short* wqkv = (const unsigned short*)d_in[1];
  const unsigned short* wo = (const unsigned short*)d_in[2];
  const unsigned short* wr = (const unsigned short*)d_in[4];
  const unsigned short* wg = (const unsigned short*)d_in[5];
  const unsigned short* wu = (const unsigned short*)d_in[6];
  const unsigned short* wd = (const unsigned short*)d_in[7];
  char* ws = (char*)d_ws;
  unsigned short* out = (unsigned short*)d_out;
  (void)hipFuncSetAttribute((const void*)k1_qkv, hipFuncAttributeMaxDynamicSharedMemorySize, K1_LDS);
  (void)hipFuncSetAttribute((const void*)k2_attn, hipFuncAttributeMaxDynamicSharedMemorySize, K2_LDS);
  (void)hipFuncSetAttribute((const void*)k3_oproj, hipFuncAttributeMaxDynamicSharedMemorySize, K3_LDS);
  k1_qkv<<<256, 1024, K1_LDS, stream>>>(x, wqkv, ws);
  k2_attn<<<256, 1024, K2_LDS, stream>>>(ws);
  k3_oproj<<<256, 512, K3_LDS, stream>>>(x, wo, wr, ws);
  k4_route<<<256, 512, K4_LDS, stream>>>(wr, wg, wu, ws, out);
  k5_down<<<64, 1024, K5_LDS, stream>>>(wd, ws, out);
}
```

```cpp
#include <hip/hip_runtime.h>
#include <hip/hip_bf16.h>
#include <stdint.h>

typedef __attribute__((ext_vector_type(8))) short bf16x8;
typedef __attribute__((ext_vector_type(4))) short s16x4;
typedef __attribute__((ext_vector_type(2))) __bf16 bf16x2_t;
typedef __attribute__((ext_vector_type(16))) float f32x16;
typedef __attribute__((ext_vector_type(4))) float f32x4;
typedef __attribute__((ext_vector_type(2))) float f32x2;
typedef __attribute__((ext_vector_type(4))) unsigned u32x4;
typedef __attribute__((ext_vector_type(2))) unsigned u32x2;

#define DI __device__ __forceinline__
#define LDS_AS __attribute__((address_space(3)))
#define MFMA32(a, b, c) __builtin_amdgcn_mfma_f32_32x32x16_bf16((a), (b), (c), 0, 0, 0)
#define MFMA16(a, b, c) __builtin_amdgcn_mfma_f32_16x16x32_bf16((a), (b), (c), 0, 0, 0)
#define WAIT_VM(n) asm volatile("s_waitcnt vmcnt(" #n ")" ::: "memory")
#define BAR() do { asm volatile("" ::: "memory"); __builtin_amdgcn_s_barrier(); asm volatile("" ::: "memory"); } while (0)
#define RAW_BAR() do { asm volatile("s_waitcnt lgkmcnt(0)" ::: "memory"); __builtin_amdgcn_s_barrier(); asm volatile("" ::: "memory"); } while (0)

#define ST_DECL
#define ST(slot)
#define ST_FLUSH(kid)

constexpr int S_ = 2048, D_ = 512, H_ = 32, F_ = 1792;
constexpr float LN_EPS = 1e-5f;

constexpr unsigned WS_Q = 0;
constexpr unsigned WS_K = WS_Q + S_ * D_ * 2;
constexpr unsigned WS_V = WS_K + S_ * D_ * 2;
constexpr unsigned WS_O = WS_V + S_ * D_ * 2;
constexpr unsigned WS_H = WS_O + S_ * D_ * 2;
constexpr unsigned WS_FA = WS_H + S_ * D_ * 2;
constexpr unsigned WS_FB = WS_FA + 16 * 64 * 8;
constexpr unsigned WS_C1 = WS_FB + 64 * 32 * 4;
constexpr unsigned WS_C2 = WS_C1 + 32 * 8 * 64 * 16;
constexpr unsigned WS_XS = WS_C2 + 32 * 2 * 16;
constexpr unsigned WS_O1 = WS_XS + 32 * 2 * 256 * 8;
constexpr unsigned WS_DD = WS_O1 + 32 * 2 * 512 * 8;
constexpr unsigned WS_EP = WS_DD + F_ * 8;
constexpr unsigned WS_END = WS_EP + 256;

DI unsigned pack_bf16(float a, float b) {
  f32x2 v = {a, b};
  bf16x2_t r = __builtin_convertvector(v, bf16x2_t);
  return __builtin_bit_cast(unsigned, r);
}
DI float bf_lo(unsigned u) { return __uint_as_float(u << 16); }
DI float bf_hi(unsigned u) { return __uint_as_float(u & 0xffff0000u); }
DI float dot2bf(unsigned a, unsigned b, float c) {
  return __builtin_amdgcn_fdot2_f32_bf16(__builtin_bit_cast(bf16x2_t, a), __builtin_bit_cast(bf16x2_t, b), c, false);
}
DI void glds16(const void* g, char* lds_wave_base) {
  __builtin_amdgcn_global_load_lds((const __attribute__((address_space(1))) unsigned*)g,
                                   (LDS_AS unsigned*)lds_wave_base, 16, 0, 0);
}
DI void glds16_sc1(const void* g, char* lds_wave_base) {
  __builtin_amdgcn_global_load_lds((const __attribute__((address_space(1))) unsigned*)g,
                                   (LDS_AS unsigned*)lds_wave_base, 16, 0, 16);
}
DI void glds4(const void* g, char* lds_wave_base) {
  __builtin_amdgcn_global_load_lds((const __attribute__((address_space(1))) unsigned*)g,
                                   (LDS_AS unsigned*)lds_wave_base, 4, 0, 0);
}
DI s16x4 tr_read(const char* p) {
  return __builtin_amdgcn_ds_read_tr16_b64_v4i16((LDS_AS s16x4*)p);
}
DI bf16x8 cat8(s16x4 lo, s16x4 hi) { return __builtin_shufflevector(lo, hi, 0, 1, 2, 3, 4, 5, 6, 7); }
template <int CTRL> DI float dpp_f(float x) {
  return __builtin_bit_cast(float, __builtin_amdgcn_update_dpp(0, __builtin_bit_cast(int, x), CTRL, 0xF, 0xF, true));
}
DI float add_x32(float v) {
  auto r = __builtin_amdgcn_permlane32_swap(__float_as_uint(v), __float_as_uint(v), false, false);
  return __uint_as_float(r[0]) + __uint_as_float(r[1]);
}
DI float max_x32(float v) {
  auto r = __builtin_amdgcn_permlane32_swap(__float_as_uint(v), __float_as_uint(v), false, false);
  return fmaxf(__uint_as_float(r[0]), __uint_as_float(r[1]));
}
DI float add_x16(float v) {
  auto r = __builtin_amdgcn_permlane16_swap(__float_as_uint(v), __float_as_uint(v), false, false);
  return __uint_as_float(r[0]) + __uint_as_float(r[1]);
}
DI float max_x16(float v) {
  auto r = __builtin_amdgcn_permlane16_swap(__float_as_uint(v), __float_as_uint(v), false, false);
  return fmaxf(__uint_as_float(r[0]), __uint_as_float(r[1]));
}
DI float add_8(float v) { v += dpp_f<0xB1>(v); v += dpp_f<0x4E>(v); v += dpp_f<0x141>(v); return v; }
DI float add_16(float v) { v = add_8(v); v += dpp_f<0x140>(v); return v; }
DI float add_64(float v) { return add_x32(add_x16(add_16(v))); }
DI float max_16(float v) {
  v = fmaxf(v, dpp_f<0xB1>(v)); v = fmaxf(v, dpp_f<0x4E>(v)); v = fmaxf(v, dpp_f<0x141>(v)); v = fmaxf(v, dpp_f<0x140>(v));
  return v;
}
DI float max_32(float v) { return max_x16(max_16(v)); }
DI float max_64(float v) { return max_x32(max_32(v)); }

DI void exp_pack(const f32x16& s, bf16x8& p0, bf16x8& p1) {
  u32x4 a, c;
#pragma unroll
  for (int e = 0; e < 4; e++) {
    a[e] = pack_bf16(__builtin_amdgcn_exp2f(s[2 * e]), __builtin_amdgcn_exp2f(s[2 * e + 1]));
    c[e] = pack_bf16(__builtin_amdgcn_exp2f(s[8 + 2 * e]), __builtin_amdgcn_exp2f(s[8 + 2 * e + 1]));
  }
  p0 = __builtin_bit_cast(bf16x8, a);
  p1 = __builtin_bit_cast(bf16x8, c);
}
#define LD8(p) (*(const bf16x8*)(p))
#define SB() __builtin_amdgcn_sched_barrier(0)
#define EX2(x) __builtin_amdgcn_exp2f(x)

template <int NS>
DI void attn_seg(const char* ka, const char* va, int vmask, const bf16x8 qf, const f32x16& cinit, f32x16& o) {
  constexpr int NT = 2 * NS;
  bf16x8 kn = LD8(ka + 1024);
  f32x16 s = MFMA32(LD8(ka), qf, cinit);
  bf16x8 v0 = LD8(va), v1 = LD8(va + 512);
  bf16x8 vprev, pprev;
#pragma unroll
  for (int j = 0; j < NT; j++) {
    if ((j & 1) == 0) {
      if (NS - 1 - (j >> 1) == 3) __builtin_amdgcn_s_setprio(3);
      else if (NS - 1 - (j >> 1) == 2) __builtin_amdgcn_s_setprio(2);
      else if (NS - 1 - (j >> 1) == 1) __builtin_amdgcn_s_setprio(1);
      else __builtin_amdgcn_s_setprio(0);
    }
    const char* vp = va + (vmask & (((j + 1) >> 1) * 16384)) + ((j + 1) & 1) * 1024;
    const char* kp = ka + ((j + 2) >> 1) * 16384 + ((j + 2) & 1) * 1024;
    float e0[8], e1[8];
    u32x4 pa, pc;
    f32x16 sNext;
    bf16x8 w0, w1, k2;
    SB();
#pragma unroll
    for (int e = 0; e < 4; e++) e0[e] = EX2(s[e]);
    SB();
    if (j > 0) o = MFMA32(vprev, pprev, o);
    SB();
#pragma unroll
    for (int e = 4; e < 8; e++) e0[e] = EX2(s[e]);
#pragma unroll
    for (int e = 0; e < 4; e++) pa[e] = pack_bf16(e0[2 * e], e0[2 * e + 1]);
    SB();
    if (j + 1 < NT) { sNext = MFMA32(kn, qf, cinit); w0 = LD8(vp); w1 = LD8(vp + 512); }
    if (j + 2 < NT) k2 = LD8(kp);
    SB();
#pragma unroll
    for (int e = 0; e < 5; e++) e1[e] = EX2(s[8 + e]);
    SB();
    o = MFMA32(v0, __builtin_bit_cast(bf16x8, pa), o);
    SB();
#pragma unroll
    for (int e = 5; e < 8; e++) e1[e] = EX2(s[8 + e]);
#pragma unroll
    for (int e = 0; e < 4; e++) pc[e] = pack_bf16(e1[2 * e], e1[2 * e + 1]);
    pprev = __builtin_bit_cast(bf16x8, pc); vprev = v1;
    if (j + 1 < NT) { s = sNext; v0 = w0; v1 = w1; }
    if (j + 2 < NT) kn = k2;
  }
  SB();
  o = MFMA32(vprev, pprev, o);
}

typedef __amdgpu_buffer_rsrc_t rsrc_t;
DI void st16(rsrc_t r, unsigned off, u32x4 v) { __builtin_amdgcn_raw_buffer_store_b128(v, r, off, 0, 16); }
DI void st8(rsrc_t r, unsigned off, u32x2 v) { __builtin_amdgcn_raw_buffer_store_b64(v, r, off, 0, 16); }
DI void st4(rsrc_t r, unsigned off, unsigned v) { __builtin_amdgcn_raw_buffer_store_b32(v, r, off, 0, 16); }
DI u32x4 ld16(rsrc_t r, unsigned off) { asm volatile("" : "+v"(off) :: "memory"); return __builtin_amdgcn_raw_buffer_load_b128(r, off, 0, 16); }
DI u32x2 ld8(rsrc_t r, unsigned off) { asm volatile("" : "+v"(off) :: "memory"); return __builtin_amdgcn_raw_buffer_load_b64(r, off, 0, 16); }
DI unsigned ld4(rsrc_t r, unsigned off) { asm volatile("" : "+v"(off) :: "memory"); return __builtin_amdgcn_raw_buffer_load_b32(r, off, 0, 16); }
#define MEMFENCE() asm volatile("" ::: "memory")
constexpr int SPIN_MAX = 100000;
#define SPIN_PAUSE() do { __builtin_amdgcn_s_sleep(2); MEMFENCE(); } while (0)

constexpr int L_RING = 0;
constexpr int P1_XB = 128 * 64 * 2, P1_WB = 64 * 96 * 2, P1_ST = P1_XB + P1_WB;
constexpr int L_P1AUX = 3 * P1_ST;
constexpr int L_RED = 65536;
constexpr int L_ONES = 135168;
constexpr int L_Q = L_ONES + 2048;
constexpr int L_SC = L_Q + 8192;
constexpr int L_MISC = L_SC + 16384;
constexpr int L_TOTAL = L_MISC + 1024;

__global__ __launch_bounds__(1024) void fused_block(const unsigned short* __restrict__ x, const unsigned short* __restrict__ wqkv,
                                                    const unsigned short* __restrict__ wo, const unsigned short* __restrict__ wr,
                                                    const unsigned short* __restrict__ wg, const unsigned short* __restrict__ wu,
                                                    const unsigned short* __restrict__ wd, char* ws,
                                                    unsigned short* __restrict__ out) {
  extern __shared__ __attribute__((aligned(16))) char smem[];
  const int tid = threadIdx.x, lane = tid & 63;
  const int wv = __builtin_amdgcn_readfirstlane(tid >> 6);
  const int b = blockIdx.x, xcd = b & 7, bi = b >> 3;
  const int hp = 2 * xcd + (bi & 1), tb = bi >> 1;
  const rsrc_t rs = __builtin_amdgcn_make_buffer_rsrc(ws, 0, WS_END, 0x00020000);
  const int r = lane & 31, h = lane >> 5;
  const unsigned ONES = 0x3f803f80u;
  float* misc = (float*)(smem + L_MISC);
  ST_DECL ST(0);
  const unsigned tag_v = ld4(rs, WS_EP);
  unsigned tag;

  {
    const int m0 = tb * 128;
    const int m = wv & 3, role = wv >> 2;
    const int xsw = (r >> 1) & 7;
    const int xrow_off = (32 * m + r) * 128;
    auto ldx = [&](int c, int ks) { return *(const bf16x8*)(smem + (c % 3) * P1_ST + xrow_off + (((2 * ks + h) ^ xsw) << 4)); };
    float* csl = (float*)(smem + L_P1AUX);
    float* str = csl + 4 * 96;
    float* stm = str + 128;
    if (role == 0) {
      if (tid < 128) {
        const unsigned o1v = ((tid & 15) == 0 || (tid & 15) == 4) ? ONES : 0u;
        *(u32x4*)(smem + L_ONES + tid * 16) = (u32x4){o1v, o1v, o1v, o1v};
      }
      const int blk = (lane >> 4) & 1, q = (lane & 15) >> 2, p = lane & 3;
      const int woff = P1_XB + (8 * h + q) * 192 + blk * 32 + p * 8;
      auto ldw = [&](int c, int ks, int sg) {
        const char* pw = smem + (c % 3) * P1_ST + woff + ks * 16 * 192 + sg * 64;
        return cat8(tr_read(pw), tr_read(pw + 4 * 192));
      };
      f32x16 aq, ak, av;
#pragma unroll
      for (int i = 0; i < 16; i++) { aq[i] = 0.f; ak[i] = 0.f; av[i] = 0.f; }
      float s0 = 0.f, s1 = 0.f, s2 = 0.f;
      tag = __builtin_amdgcn_readfirstlane(tag_v) + 1u;
      RAW_BAR();
      ST(1);
      bf16x8 xf = ldx(0, 0), wq = ldw(0, 0, 0), wk = ldw(0, 0, 1), wvv = ldw(0, 0, 2);
#pragma unroll
      for (int c = 0; c < 8; c++) {
#pragma unroll
        for (int ks = 0; ks < 4; ks++) {
          const int nc = ks == 3 ? c + 1 : c, nk = (ks + 1) & 3;
          bf16x8 nx, nq, nkk, nv;
          if (nc < 8) { nx = ldx(nc, nk); nq = ldw(nc, nk, 0); nkk = ldw(nc, nk, 1); nv = ldw(nc, nk, 2); }
          SB();
          aq = MFMA32(wq, xf, aq);
          ak = MFMA32(wk, xf, ak);
          av = MFMA32(xf, wvv, av);
          if (ks == m) {
            u32x4 a0 = __builtin_bit_cast(u32x4, wq), a1 = __builtin_bit_cast(u32x4, wk), a2 = __builtin_bit_cast(u32x4, wvv);
#pragma unroll
            for (int e = 0; e < 4; e++) { s0 = dot2bf(a0[e], ONES, s0); s1 = dot2bf(a1[e], ONES, s1); s2 = dot2bf(a2[e], ONES, s2); }
          }
          SB();
          if (nc < 8) { xf = nx; wq = nq; wk = nkk; wvv = nv; }
        }
        if (c < 6) BAR();
      }
      ST(2);
      {
        const float c0 = add_x32(s0), c1 = add_x32(s1), c2 = add_x32(s2);
        if (h == 0) { csl[m * 96 + r] = c0; csl[m * 96 + 32 + r] = c1; csl[m * 96 + 64 + r] = c2; }
      }
      RAW_BAR();
      const int T = tb * 4 + m;
      const float mean = stm[32 * m + r];
      const float rsd = str[32 * m + r];
      const float rq = rsd * (0.25f * 1.4426950408889634f);
      float vq[16], vk[16];
#pragma unroll
      for (int g = 0; g < 4; g++) {
        f32x4 cq = {0.f, 0.f, 0.f, 0.f}, ck = {0.f, 0.f, 0.f, 0.f};
#pragma unroll
        for (int ww = 0; ww < 4; ww++) {
          cq += *(const f32x4*)(csl + ww * 96 + 8 * g + 4 * h);
          ck += *(const f32x4*)(csl + ww * 96 + 32 + 8 * g + 4 * h);
        }
#pragma unroll
        for (int e = 0; e < 4; e++) {
          vq[4 * g + e] = (aq[4 * g + e] - mean * cq[e]) * rq;
          vk[4 * g + e] = (ak[4 * g + e] - mean * ck[e]) * rsd;
        }
      }
#pragma unroll
      for (int hh = 0; hh < 2; hh++) {
        u32x4 pq, pk;
        float n2 = 0.f;
#pragma unroll
        for (int e = 0; e < 4; e++) {
          pq[e] = pack_bf16(vq[8 * hh + 2 * e], vq[8 * hh + 2 * e + 1]);
          pk[e] = pack_bf16(vk[8 * hh + 2 * e], vk[8 * hh + 2 * e + 1]);
          n2 = dot2bf(pk[e], pk[e], n2);
        }
        st16(rs, WS_K + ((2 * hp + hh) * 64 + T) * 1024 + lane * 16, pk);
        *(u32x4*)(smem + L_RING + hh * 8192 + m * 1024 + lane * 16) = pk;
        *(u32x4*)(smem + L_Q + (hh * 4 + m) * 1024 + lane * 16) = pq;
        n2 = max_32(add_x32(n2));
        if (lane == 0) misc[hh * 4 + m] = n2;
      }
      const float cv = csl[64 + r] + csl[96 + 64 + r] + csl[192 + 64 + r] + csl[288 + 64 + r];
      float vv[16];
#pragma unroll
      for (int g = 0; g < 4; g++) {
        const f32x4 a4 = *(const f32x4*)(str + 32 * m + 8 * g + 4 * h);
        const f32x4 b4 = *(const f32x4*)(stm + 32 * m + 8 * g + 4 * h);
#pragma unroll
        for (int e = 0; e < 4; e++) vv[4 * g + e] = (av[4 * g + e] - b4[e] * cv) * a4[e];
      }
      const int hh = r >> 4, d = r & 15;
#pragma unroll
      for (int s = 0; s < 2; s++) {
        u32x4 pv;
#pragma unroll
        for (int e = 0; e < 4; e++) pv[e] = pack_bf16(vv[8 * s + 2 * e], vv[8 * s + 2 * e + 1]);
        st16(rs, WS_V + ((2 * hp + hh) * 64 + T) * 1024 + s * 512 + h * 256 + d * 16, pv);
        *(u32x4*)(smem + L_RING + hh * 8192 + 4096 + m * 1024 + s * 512 + h * 256 + d * 16) = pv;
      }
      RAW_BAR();
    } else {
      const int slot = (wv - 4) * 64 + lane;
      const int kr = slot / 12, sq = slot - kr * 12;
      const unsigned short* wsrc = wqkv + (size_t)kr * (3 * D_) + (sq >> 2) * D_ + hp * 32 + (sq & 3) * 8;
      const int wdst = P1_XB + slot * 16;
      if (role == 1) {
        u32x4 wst[8];
#pragma unroll
        for (int c = 0; c < 8; c++) wst[c] = *(const u32x4*)(wsrc + (size_t)c * 64 * (3 * D_));
        MEMFENCE();
        tag = __builtin_amdgcn_readfirstlane(tag_v) + 1u;
        float s0 = 0.f, s1 = 0.f, q0 = 0.f, q1 = 0.f;
        *(u32x4*)(smem + wdst) = wst[0];
        *(u32x4*)(smem + P1_ST + wdst) = wst[1];
        RAW_BAR();
#pragma unroll
        for (int c = 0; c < 8; c++) {
          if (c + 2 < 8) *(u32x4*)(smem + ((c + 2) % 3) * P1_ST + wdst) = wst[c + 2];
#pragma unroll
          for (int ks = 0; ks < 4; ks++) {
            u32x4 xu = __builtin_bit_cast(u32x4, ldx(c, ks));
            s0 = dot2bf(xu[0], ONES, s0); s1 = dot2bf(xu[1], ONES, s1);
            s0 = dot2bf(xu[2], ONES, s0); s1 = dot2bf(xu[3], ONES, s1);
            q0 = dot2bf(xu[0], xu[0], q0); q1 = dot2bf(xu[1], xu[1], q1);
            q0 = dot2bf(xu[2], xu[2], q0); q1 = dot2bf(xu[3], xu[3], q1);
          }
          if (c < 6) RAW_BAR();
        }
        const float sum = add_x32(s0 + s1), sqs = add_x32(q0 + q1);
        const float mean = sum * (1.f / D_);
        const float var = fmaxf(sqs * (1.f / D_) - mean * mean, 0.f);
        if (h == 0) { str[32 * m + r] = rsqrtf(var + LN_EPS); stm[32 * m + r] = mean; }
        RAW_BAR();
        RAW_BAR();
      } else {
        u32x4 wst[8], xst[8][2];
        const unsigned short* xsrc[2];
        int xdst[2];
#pragma unroll
        for (int i = 0; i < 2; i++) {
          const int row = ((wv - 8) * 2 + i) * 8 + (lane >> 3);
          xsrc[i] = x + (size_t)(m0 + row) * D_ + (lane & 7) * 8;
          xdst[i] = row * 128 + (((lane & 7) ^ ((row >> 1) & 7)) << 4);
        }
#pragma unroll
        for (int c = 0; c < 8; c++) {
          xst[c][0] = *(const u32x4*)(xsrc[0] + c * 64);
          xst[c][1] = *(const u32x4*)(xsrc[1] + c * 64);
          wst[c] = *(const u32x4*)(wsrc + (size_t)c * 64 * (3 * D_));
        }
        MEMFENCE();
        tag = __builtin_amdgcn_readfirstlane(tag_v) + 1u;
        auto put = [&](int c) {
          char* base = smem + (c % 3) * P1_ST;
          *(u32x4*)(base + xdst[0]) = xst[c][0];
          *(u32x4*)(base + xdst[1]) = xst[c][1];
          *(u32x4*)(base + wdst) = wst[c];
        };
        put(0); put(1);
        RAW_BAR();
#pragma unroll
        for (int c = 0; c < 6; c++) { put(c + 2); RAW_BAR(); }
        RAW_BAR();
        RAW_BAR();
      }
    }
  }
  ST(3);

  u32x4 xres, wrv;
  {
    const int a = wv >> 3, kh = (wv >> 2) & 1, qt = wv & 3;
    const int head = 2 * hp + a;
    const bf16x8 qf = LD8(smem + L_Q + (a * 4 + qt) * 1024 + lane * 16);
    float q2;
    {
      u32x4 qu = __builtin_bit_cast(u32x4, qf);
      q2 = dot2bf(qu[0], qu[0], 0.f);
      q2 = dot2bf(qu[1], qu[1], q2); q2 = dot2bf(qu[2], qu[2], q2); q2 = dot2bf(qu[3], qu[3], q2);
      q2 = add_x32(q2);
    }
    const f32x4 kl = *(const f32x4*)(misc + a * 4);
    const float m0row = __builtin_amdgcn_sqrtf(q2 * fmaxf(fmaxf(kl[0], kl[1]), fmaxf(kl[2], kl[3]))) * 1.001f;
    const int kbase = L_RING + a * 8192 + kh * 2048 + lane * 16;
    const int vl = (lane >> 5) * 256 + (lane & 15) * 16;
    const bool ones_lane = (lane & 16) != 0;
    const int vbase = ones_lane ? (L_ONES + vl) : (L_RING + a * 8192 + 4096 + kh * 2048 + vl);
    const int vmask = ones_lane ? 0 : -1;
    f32x16 cinit, o;
#pragma unroll
    for (int i = 0; i < 16; i++) { cinit[i] = -m0row; o[i] = 0.f; }
    attn_seg<1>(smem + kbase, smem + vbase, vmask, qf, cinit, o);
    if (wv < 4) {
      WAIT_VM(0);
      if (lane == 0) {
        const unsigned k0 = (__float_as_uint(misc[wv]) + 0xffffu) >> 16, k1 = (__float_as_uint(misc[4 + wv]) + 0xffffu) >> 16;
        st8(rs, WS_FA + (hp * 64 + tb * 4 + wv) * 8, (u32x2){k0 | (k1 << 16), tag});
      }
    } else if (wv == 4) {
      u32x2 f;
#pragma unroll 1
      for (int it = 0; it < SPIN_MAX; it++) {
        f = ld8(rs, WS_FA + (hp * 64 + lane) * 8);
        if (__all(f[1] == tag)) break;
        SPIN_PAUSE();
      }
      const float k0 = max_64(bf_lo(f[0])), k1 = max_64(bf_hi(f[0]));
      if (lane == 0) { misc[8] = k0; misc[9] = k1; }
    }
    RAW_BAR();
    ST(4);
    const char* gsrc = ws + (((wv >> 2) & 1) ? WS_V : WS_K) + (size_t)(head * 64 + (wv & 3)) * 1024 + lane * 16;
    auto fill = [&](int s) {
      glds16_sc1(gsrc + (size_t)((tb + s) & 15) * 4096, smem + L_RING + (s & 7) * 16384 + wv * 1024);
    };
#pragma unroll
    for (int s = 1; s < 8; s++) { fill(s); if (s == 3) BAR(); }
    {
      const float m1row = fmaxf(__builtin_amdgcn_sqrtf(q2 * misc[8 + a]) * 1.001f, m0row);
      const float f = EX2(m0row - m1row);
#pragma unroll
      for (int i = 0; i < 9; i++) o[i] *= f;
#pragma unroll
      for (int i = 0; i < 16; i++) cinit[i] = -m1row;
    }
    WAIT_VM(4);
    RAW_BAR();
    ST(5);
    attn_seg<3>(smem + kbase + 16384, smem + vbase + (vmask & 16384), vmask, qf, cinit, o);
#pragma unroll 1
    for (int u = 1; u < 4; u++) {
      WAIT_VM(0);
      RAW_BAR();
      if (u < 3) { fill(4 * u + 4); fill(4 * u + 5); fill(4 * u + 6); fill(4 * u + 7); }
      else {
        const int rr = lane >> 3;
        const int c = (lane & 7) ^ (((rr >> 1) & 1) << 2);
        const unsigned short* src = wo + (size_t)(32 * wv + rr) * D_ + (hp >> 1) * 64 + c * 8;
#pragma unroll
        for (int i = 0; i < 4; i++) glds16(src + (size_t)i * 8 * D_, smem + L_RING + (4 * wv + i) * 1024);
      }
      const int so = (u & 1) * 65536;
      attn_seg<4>(smem + kbase + so, smem + vbase + (vmask & so), vmask, qf, cinit, o);
    }
    __builtin_amdgcn_s_setprio(0);
    ST(6);
    float* lx = (float*)(smem + L_Q);
    float* mx = (float*)(smem + L_Q + 4096);
    float* sc = (float*)(smem + L_SC) + (a * 4 + qt) * 512;
    const int pw = wv ^ 4;
    lx[wv * 64 + lane] = o[8];
    if (kh == 1) {
#pragma unroll
      for (int i = 0; i < 8; i++) sc[i * 64 + lane] = o[i];
    }
    if (tid == 0) misc[10] = 0.f;
    WAIT_VM(0);
    RAW_BAR();
    float ltot = o[8] + lx[pw * 64 + lane];
    const bool bad = !(ltot >= 9.094947e-13f);
    if (__any(bad)) misc[10] = 1.f;
    RAW_BAR();
    if (misc[10] != 0.f) {
      const unsigned gk = WS_K + (head * 64 + kh * 32) * 1024 + lane * 16;
      const unsigned gv = WS_V + (head * 64 + kh * 32) * 1024 + vl;
      float mt = -INFINITY;
      f32x16 zero;
#pragma unroll
      for (int i = 0; i < 16; i++) zero[i] = 0.f;
#pragma unroll 1
      for (int t = 0; t < 32; t++) {
        f32x16 sv = MFMA32(__builtin_bit_cast(bf16x8, ld16(rs, gk + t * 1024)), qf, zero);
#pragma unroll
        for (int i = 0; i < 16; i++) mt = fmaxf(mt, sv[i]);
      }
      mt = max_x32(mt);
      mx[wv * 64 + lane] = mt;
      RAW_BAR();
      mt = fmaxf(mt, mx[pw * 64 + lane]);
      const bf16x8 cst = LD8(smem + L_ONES + vl);
#pragma unroll
      for (int i = 0; i < 16; i++) { cinit[i] = -mt; o[i] = 0.f; }
#pragma unroll 1
      for (int t = 0; t < 32; t++) {
        f32x16 sv = MFMA32(__builtin_bit_cast(bf16x8, ld16(rs, gk + t * 1024)), qf, cinit);
        bf16x8 w0 = __builtin_bit_cast(bf16x8, ld16(rs, gv + t * 1024)), w1 = __builtin_bit_cast(bf16x8, ld16(rs, gv + t * 1024 + 512));
        if (ones_lane) { w0 = cst; w1 = cst; }
        bf16x8 p0, p1;
        exp_pack(sv, p0, p1);
        o = MFMA32(w0, p0, o);
        o = MFMA32(w1, p1, o);
      }
      lx[wv * 64 + lane] = o[8];
      if (kh == 1) {
#pragma unroll
        for (int i = 0; i < 8; i++) sc[i * 64 + lane] = o[i];
      }
      RAW_BAR();
      ltot = o[8] + lx[pw * 64 + lane];
    }
    if (kh == 0) {
      const float inv = 1.f / ltot;
      u32x4 po;
#pragma unroll
      for (int e = 0; e < 4; e++) {
        const float x0 = (o[2 * e] + sc[(2 * e) * 64 + lane]) * inv;
        const float x1 = (o[2 * e + 1] + sc[(2 * e + 1) * 64 + lane]) * inv;
        po[e] = pack_bf16(x0, x1);
      }
      st16(rs, WS_O + ((tb * 4 + qt) * 32 + head) * 1024 + lane * 16, po);
      WAIT_VM(0);
      if (lane == 0) st4(rs, WS_FB + ((tb * 4 + qt) * 32 + head) * 4, tag);
    }
  }
  ST(7);

  const int rb = tb * 2 + (hp & 1), ct = hp >> 1;
  const bool is_up = bi < 28;
  const int cgp = xcd * 28 + bi;
  const int dgp = xcd * 4 + (bi - 28);
  const int tok = (tid >> 3) & 63, cg = tid & 7;
  float o1[8];
  int I1 = 0, I2 = 0;
  {
    float* red = (float*)(smem + L_RED);
    if (wv < 8) {
      xres = *(const u32x4*)(x + (size_t)(rb * 64 + tok) * D_ + ct * 64 + cg * 8);
      wrv = *(const u32x4*)(wr + ct * 64 + cg * 8);
      const int m = wv & 1, kq = wv >> 1;
      const int T = rb * 2 + m;
#pragma unroll 1
      for (int it = 0; it < SPIN_MAX; it++) {
        const unsigned f = ld4(rs, WS_FB + (T * 32 + kq * 8 + (lane & 7)) * 4);
        if (__all(f == tag)) break;
        SPIN_PAUSE();
      }
      MEMFENCE();
      ST(8);
      u32x4 of[8];
#pragma unroll
      for (int i = 0; i < 8; i++) of[i] = ld16(rs, WS_O + (T * 32 + kq * 8 + i) * 1024 + lane * 16);
      MEMFENCE();
      const int blk = (lane >> 4) & 1, q = (lane & 15) >> 2, p = lane & 3;
      f32x16 acc[2];
#pragma unroll
      for (int i = 0; i < 16; i++) { acc[0][i] = 0.f; acc[1][i] = 0.f; }
      const int rsw = ((q >> 1) & 1) << 2;
#pragma unroll
      for (int i = 0; i < 8; i++) {
        const char* rowp = smem + L_RING + (kq * 128 + i * 16 + 4 * h + q) * 128;
#pragma unroll
        for (int n = 0; n < 2; n++) {
          const int chunk = (n * 4 + blk * 2 + (p >> 1)) ^ rsw;
          const char* pw = rowp + chunk * 16 + (p & 1) * 8;
          bf16x8 wf = cat8(tr_read(pw), tr_read(pw + 8 * 128));
          acc[n] = MFMA32(wf, __builtin_bit_cast(bf16x8, of[i]), acc[n]);
        }
      }
      float* dst = red + (kq * 64 + m * 32 + r) * 68 + 4 * h;
#pragma unroll
      for (int n = 0; n < 2; n++)
#pragma unroll
        for (int g = 0; g < 4; g++)
          *(f32x4*)(dst + n * 32 + 8 * g) = (f32x4){acc[n][4 * g], acc[n][4 * g + 1], acc[n][4 * g + 2], acc[n][4 * g + 3]};
    }
    RAW_BAR();
    ST(9);
    if (wv >= 8) {
      if (is_up) {
#pragma unroll
        for (int i = 0; i < 2; i++) {
          const int slot = ((wv - 8) * 2 + i) * 64 + lane;
          const unsigned short* src = ((slot & 1) ? wu : wg) + (size_t)(slot >> 1) * F_ + cgp * 8;
          glds16(src, smem + L_SC + ((wv - 8) * 2 + i) * 1024);
        }
      } else {
#pragma unroll
        for (int i = 0; i < 7; i++) {
          const int slot = ((wv - 8) * 7 + i) * 64 + lane;
          glds16(wd + (size_t)(slot >> 1) * D_ + dgp * 16 + (slot & 1) * 8, smem + L_RING + ((wv - 8) * 7 + i) * 1024);
        }
      }
    }
    float rv = -INFINITY;
    u32x4 hn4;
    if (wv < 8) {
      f32x4 a0 = {0.f, 0.f, 0.f, 0.f}, a1 = {0.f, 0.f, 0.f, 0.f};
#pragma unroll
      for (int k = 0; k < 4; k++) {
        const float* src = red + (k * 64 + tok) * 68 + cg * 8;
        a0 += *(const f32x4*)src;
        a1 += *(const f32x4*)(src + 4);
      }
      u32x4 hq;
      hq[0] = pack_bf16(a0[0] + bf_lo(xres[0]), a0[1] + bf_hi(xres[0]));
      hq[1] = pack_bf16(a0[2] + bf_lo(xres[1]), a0[3] + bf_hi(xres[1]));
      hq[2] = pack_bf16(a1[0] + bf_lo(xres[2]), a1[1] + bf_hi(xres[2]));
      hq[3] = pack_bf16(a1[2] + bf_lo(xres[3]), a1[3] + bf_hi(xres[3]));
      float s1 = 0.f, s2 = 0.f, s3 = 0.f;
#pragma unroll
      for (int e = 0; e < 4; e++) {
        s1 = dot2bf(hq[e], ONES, s1);
        s2 = dot2bf(hq[e], hq[e], s2);
        s3 = dot2bf(hq[e], wrv[e], s3);
      }
      s1 = add_8(s1); s2 = add_8(s2); s3 = add_8(s3);
      if (cg == 0) st16(rs, WS_C1 + ((rb * 8 + ct) * 64 + tok) * 16, (u32x4){__float_as_uint(s1), __float_as_uint(s2), __float_as_uint(s3), tag});
      float wsum;
      {
        const u32x4 w4 = *(const u32x4*)(wr + lane * 8);
        wsum = dot2bf(w4[0], ONES, 0.f); wsum = dot2bf(w4[1], ONES, wsum);
        wsum = dot2bf(w4[2], ONES, wsum); wsum = dot2bf(w4[3], ONES, wsum);
        wsum = add_64(wsum);
      }
      u32x4 g4;
#pragma unroll 1
      for (int it = 0; it < SPIN_MAX; it++) {
        g4 = ld16(rs, WS_C1 + ((rb * 8 + cg) * 64 + tok) * 16);
        if (__all(g4[3] == tag)) break;
        SPIN_PAUSE();
      }
      ST(10);
      const float t1 = add_8(__uint_as_float(g4[0])), t2 = add_8(__uint_as_float(g4[1])), t3 = add_8(__uint_as_float(g4[2]));
      const float mean = t1 * (1.f / D_);
      const float rstd = rsqrtf(fmaxf(t2 * (1.f / D_) - mean * mean, 0.f) + LN_EPS);
      rv = rstd * (t3 - mean * wsum);
#pragma unroll
      for (int e = 0; e < 4; e++) {
        const float h0 = bf_lo(hq[e]), h1 = bf_hi(hq[e]);
        const float n0 = (h0 - mean) * rstd, n1 = (h1 - mean) * rstd;
        hn4[e] = pack_bf16(n0, n1);
        o1[2 * e] = h0 + n0; o1[2 * e + 1] = h1 + n1;
      }
      const float M1 = max_64(rv);
      unsigned long long bm = __ballot(rv == M1);
      const int t1i = __builtin_amdgcn_readlane(tok, bm ? __builtin_ctzll(bm) : 0);
      const float c2 = (tok == t1i) ? -INFINITY : rv;
      const float M2 = max_64(c2);
      bm = __ballot(c2 == M2 && tok != t1i);
      const int t2i = __builtin_amdgcn_readlane(tok, bm ? __builtin_ctzll(bm) : 0);
      if (lane == 0) *(f32x4*)(misc + 16 + wv * 4) = (f32x4){M1, __int_as_float(t1i), M2, __int_as_float(t2i)};
    }
    RAW_BAR();
    if (wv < 8) {
      const float cv = misc[16 + ((lane & 15) >> 1) * 4 + (lane & 1) * 2];
      const int ci = __float_as_int(misc[16 + ((lane & 15) >> 1) * 4 + (lane & 1) * 2 + 1]);
      const float M1 = max_16(cv);
      unsigned long long bm = __ballot(cv == M1);
      I1 = __builtin_amdgcn_readlane(ci, bm ? __builtin_ctzll(bm) : 0);
      const float c2 = (ci == I1) ? -INFINITY : cv;
      const float M2 = max_16(c2);
      bm = __ballot(c2 == M2 && ci != I1);
      I2 = __builtin_amdgcn_readlane(ci, bm ? __builtin_ctzll(bm) : 0);
      if (I2 == I1) I2 = I1 ^ 1;
      if (tok == I1 || tok == I2) {
        const int cd = rb * 2 + (tok == I2 ? 1 : 0);
        const unsigned xo = WS_XS + (cd * 256 + ct * 32 + cg * 4) * 8;
        st16(rs, xo, (u32x4){hn4[0], tag, hn4[1], tag});
        st16(rs, xo + 16, (u32x4){hn4[2], tag, hn4[3], tag});
        const unsigned oo = WS_O1 + (cd * 512 + ct * 64 + cg * 8) * 8;
#pragma unroll
        for (int e = 0; e < 4; e++)
          st16(rs, oo + e * 16, (u32x4){__float_as_uint(o1[2 * e]), tag, __float_as_uint(o1[2 * e + 1]), tag});
        if (ct == 0 && cg == 0) st16(rs, WS_C2 + cd * 16, (u32x4){__float_as_uint(rv), (unsigned)(rb * 64 + tok), 0u, tag});
      } else {
        u32x4 ov;
#pragma unroll
        for (int e = 0; e < 4; e++) ov[e] = pack_bf16(o1[2 * e], o1[2 * e + 1]);
        *(u32x4*)(out + (size_t)(rb * 64 + tok) * D_ + ct * 64 + cg * 8) = ov;
      }
    } else if (wv == 8) {
      u32x4 g4;
#pragma unroll 1
      for (int it = 0; it < SPIN_MAX; it++) {
        g4 = ld16(rs, WS_C2 + lane * 16);
        if (__all(g4[3] == tag)) break;
        SPIN_PAUSE();
      }
      const float cv = __uint_as_float(g4[0]);
      const float M1 = max_64(cv);
      unsigned long long bm = __ballot(cv == M1);
      const int l1 = bm ? __builtin_ctzll(bm) : 0;
      const float c2 = (lane == l1) ? -INFINITY : cv;
      const float M2 = max_64(c2);
      bm = __ballot(c2 == M2 && lane != l1);
      const int l2 = bm ? __builtin_ctzll(bm) : (l1 ^ 1);
      const int r1 = __builtin_amdgcn_readlane((int)g4[1], l1), r2 = __builtin_amdgcn_readlane((int)g4[1], l2);
      if (lane == 0) {
        *(f32x4*)(misc + 64) = (f32x4){__int_as_float(l1), __int_as_float(l2), __int_as_float(r1 & (S_ - 1)), __int_as_float(r2 & (S_ - 1))};
        misc[68] = M1; misc[69] = M2;
        if (b == 0) st4(rs, WS_EP, tag);
      }
    }
    ST(11);
    if (wv >= 8) WAIT_VM(0);
    RAW_BAR();
    ST(12);
  }
  const int cdA = __float_as_int(misc[64]), cdB = __float_as_int(misc[65]);
  const int rowA = __float_as_int(misc[66]), rowB = __float_as_int(misc[67]);
  if (wv < 8 && (tok == I1 || tok == I2)) {
    const int grow = rb * 64 + tok;
    if (grow != rowA && grow != rowB) {
      u32x4 ov;
#pragma unroll
      for (int e = 0; e < 4; e++) ov[e] = pack_bf16(o1[2 * e], o1[2 * e + 1]);
      *(u32x4*)(out + (size_t)grow * D_ + ct * 64 + cg * 8) = ov;
    }
  }
  if (is_up) {
    char* xs = smem + L_RED;
    if (wv >= 8 && wv < 12) {
      const int t = tid - 512, k = t >> 7, g2 = t & 127;
      const unsigned off = WS_XS + ((k ? cdB : cdA) * 256 + g2 * 2) * 8;
      u32x4 g4;
#pragma unroll 1
      for (int it = 0; it < SPIN_MAX; it++) {
        g4 = ld16(rs, off);
        if (__all(g4[1] == tag && g4[3] == tag)) break;
        SPIN_PAUSE();
      }
      *(u32x2*)(xs + k * 1024 + g2 * 8) = (u32x2){g4[0], g4[2]};
    }
    RAW_BAR();
    ST(13);
    if (wv == 8) {
      const int g = lane >> 4, q = (lane & 15) >> 2, p = lane & 3;
      const char* xa = xs + (lane & 1) * 1024 + g * 16;
      const char* wa = smem + L_SC + (8 * g + q) * 32 + p * 8;
      f32x4 c0 = {0.f, 0.f, 0.f, 0.f}, c1 = {0.f, 0.f, 0.f, 0.f};
#pragma unroll
      for (int s = 0; s < 16; s += 2) {
        c0 = MFMA16(LD8(xa + s * 64), cat8(tr_read(wa + s * 1024), tr_read(wa + s * 1024 + 128)), c0);
        c1 = MFMA16(LD8(xa + s * 64 + 64), cat8(tr_read(wa + s * 1024 + 1024), tr_read(wa + s * 1024 + 1152)), c1);
      }
      const float g0 = c0[0] + c1[0], g1 = c0[1] + c1[1];
      const float u0 = dpp_f<0x128>(g0), u1 = dpp_f<0x128>(g1);
      if (lane < 8) {
        const unsigned gp = pack_bf16(g0, g1), up = pack_bf16(u0, u1);
        const float ga = bf_lo(gp), gb = bf_hi(gp);
        const unsigned sp = pack_bf16(ga / (1.f + __expf(-ga)), gb / (1.f + __expf(-gb)));
        const unsigned ap = pack_bf16(bf_lo(sp) * bf_lo(up), bf_hi(sp) * bf_hi(up));
        st8(rs, WS_DD + (cgp * 8 + lane) * 8, (u32x2){ap, tag});
      }
    }
  } else {
    unsigned short* act = (unsigned short*)(smem + L_RED);
    float* red2 = (float*)(smem + L_RED + 8192);
    float base = 0.f;
    if (wv == 15 && lane < 32) {
      const unsigned off = WS_O1 + (((lane >> 4) ? cdB : cdA) * 512 + dgp * 16 + (lane & 15)) * 8;
      u32x2 g2;
#pragma unroll 1
      for (int it = 0; it < SPIN_MAX; it++) {
        g2 = ld8(rs, off);
        if (__all(g2[1] == tag)) break;
        SPIN_PAUSE();
      }
      base = __uint_as_float(g2[0]);
    }
    if (wv < 14) {
      u32x4 g4;
#pragma unroll 1
      for (int it = 0; it < SPIN_MAX; it++) {
        g4 = ld16(rs, WS_DD + tid * 16);
        if (__all(g4[1] == tag && g4[3] == tag)) break;
        SPIN_PAUSE();
      }
      *(unsigned*)(act + tid * 2) = (g4[0] & 0xffffu) | (g4[2] << 16);
      *(unsigned*)(act + F_ + tid * 2) = (g4[0] >> 16) | (g4[2] & 0xffff0000u);
    }
    RAW_BAR();
    ST(14);
    if (wv < 8) {
      const int g = lane >> 4, q = (lane & 15) >> 2, p = lane & 3;
      const char* xa = (const char*)act + (lane & 1) * (F_ * 2) + g * 16 + wv * 7 * 64;
      const char* wa = smem + L_RING + (wv * 7 * 32 + 8 * g + q) * 32 + p * 8;
      f32x4 c0 = {0.f, 0.f, 0.f, 0.f};
#pragma unroll
      for (int s = 0; s < 7; s++)
        c0 = MFMA16(LD8(xa + s * 64), cat8(tr_read(wa + s * 1024), tr_read(wa + s * 1024 + 128)), c0);
      if (lane < 16) { red2[wv * 32 + lane] = c0[0]; red2[wv * 32 + 16 + lane] = c0[1]; }
    }
    RAW_BAR();
    if (wv == 15 && lane < 32) {
      float y = 0.f;
#pragma unroll
      for (int k = 0; k < 8; k++) y += red2[k * 32 + lane];
      const float rsel = misc[68 + (lane >> 4)];
      const float gate = 1.f / (1.f + __expf(-rsel));
      const int grow = (lane >> 4) ? rowB : rowA;
      out[(size_t)grow * D_ + dgp * 16 + (lane & 15)] = (unsigned short)(pack_bf16(base + y * gate, 0.f) & 0xffffu);
    }
  }
  ST(15);
  ST_FLUSH(0);
}

extern "C" void kernel_launch(void* const* d_in, const int* in_sizes, int n_in,
                              void* d_out, int out_size, void* d_ws, size_t ws_size,
                              hipStream_t stream) {
  (void)hipFuncSetAttribute((const void*)fused_block, hipFuncAttributeMaxDynamicSharedMemorySize, L_TOTAL);
  fused_block<<<256, 1024, L_TOTAL, stream>>>((const unsigned short*)d_in[0], (const unsigned short*)d_in[1],
                                             (const unsigned short*)d_in[2], (const unsigned short*)d_in[4],
                                             (const unsigned short*)d_in[5], (const unsigned short*)d_in[6],
                                             (const unsigned short*)d_in[7], (char*)d_ws, (unsigned short*)d_out);
}
```

```cpp
#include <hip/hip_runtime.h>
#include <hip/hip_bf16.h>
#include <stdint.h>

typedef __attribute__((ext_vector_type(8))) short bf16x8;
typedef __attribute__((ext_vector_type(4))) short s16x4;
typedef __attribute__((ext_vector_type(2))) __bf16 bf16x2_t;
typedef __attribute__((ext_vector_type(16))) float f32x16;
typedef __attribute__((ext_vector_type(4))) float f32x4;
typedef __attribute__((ext_vector_type(2))) float f32x2;
typedef __attribute__((ext_vector_type(4))) unsigned u32x4;
typedef __attribute__((ext_vector_type(2))) unsigned u32x2;

#define DI __device__ __forceinline__
#define LDS_AS __attribute__((address_space(3)))
#define MFMA32(a, b, c) __builtin_amdgcn_mfma_f32_32x32x16_bf16((a), (b), (c), 0, 0, 0)
#define MFMA16(a, b, c) __builtin_amdgcn_mfma_f32_16x16x32_bf16((a), (b), (c), 0, 0, 0)
#define WAIT_VM(n) asm volatile("s_waitcnt vmcnt(" #n ")" ::: "memory")
#define BAR() do { asm volatile("" ::: "memory"); __builtin_amdgcn_s_barrier(); asm volatile("" ::: "memory"); } while (0)
#define RAW_BAR() do { asm volatile("s_waitcnt lgkmcnt(0)" ::: "memory"); __builtin_amdgcn_s_barrier(); asm volatile("" ::: "memory"); } while (0)

#define ST_DECL
#define ST(slot)
#define ST_FLUSH(kid)
#define STP(slot)
#define STL(slot)

constexpr int S_ = 2048, D_ = 512, H_ = 32, F_ = 1792;
constexpr float LN_EPS = 1e-5f;

constexpr unsigned WS_Q = 0;
constexpr unsigned WS_K = WS_Q + S_ * D_ * 2;
constexpr unsigned WS_V = WS_K + S_ * D_ * 2;
constexpr unsigned WS_O = WS_V + S_ * D_ * 2;
constexpr unsigned WS_H = WS_O + S_ * D_ * 2;
constexpr unsigned WS_FA = WS_H + S_ * D_ * 2;
constexpr unsigned WS_FB = WS_FA + 16 * 64 * 8;
constexpr unsigned WS_C1 = WS_FB + 64 * 32 * 4;
constexpr unsigned WS_C2 = WS_C1 + 32 * 8 * 64 * 16;
constexpr unsigned WS_XS = WS_C2 + 32 * 2 * 16;
constexpr unsigned WS_O1 = WS_XS + 32 * 2 * 256 * 8;
constexpr unsigned WS_DD = WS_O1 + 32 * 2 * 512 * 8;
constexpr unsigned WS_EP = WS_DD + F_ * 8;
constexpr unsigned WS_END = WS_EP + 256;

DI unsigned pack_bf16(float a, float b) {
  f32x2 v = {a, b};
  bf16x2_t r = __builtin_convertvector(v, bf16x2_t);
  return __builtin_bit_cast(unsigned, r);
}
DI float bf_lo(unsigned u) { return __uint_as_float(u << 16); }
DI float bf_hi(unsigned u) { return __uint_as_float(u & 0xffff0000u); }
DI float dot2bf(unsigned a, unsigned b, float c) {
  return __builtin_amdgcn_fdot2_f32_bf16(__builtin_bit_cast(bf16x2_t, a), __builtin_bit_cast(bf16x2_t, b), c, false);
}
DI void glds16(const void* g, unsigned lds_base) {
  unsigned keep;
  asm volatile("s_mov_b32 %0, m0\n\ts_mov_b32 m0, %2\n\ts_nop 0\n\tglobal_load_lds_dwordx4 %1, off\n\ts_mov_b32 m0, %0"
               : "=&s"(keep) : "v"(g), "s"(lds_base) : "memory");
}
DI void glds16_sc1(const void* g, unsigned lds_base) {
  unsigned keep;
  asm volatile("s_mov_b32 %0, m0\n\ts_mov_b32 m0, %2\n\ts_nop 0\n\tglobal_load_lds_dwordx4 %1, off sc1\n\ts_mov_b32 m0, %0"
               : "=&s"(keep) : "v"(g), "s"(lds_base) : "memory");
}
DI s16x4 tr_read(const char* p) {
  return __builtin_amdgcn_ds_read_tr16_b64_v4i16((LDS_AS s16x4*)(size_t)(unsigned)(size_t)p);
}
DI bf16x8 cat8(s16x4 lo, s16x4 hi) { return __builtin_shufflevector(lo, hi, 0, 1, 2, 3, 4, 5, 6, 7); }
template <int CTRL> DI float dpp_f(float x) {
  return __builtin_bit_cast(float, __builtin_amdgcn_update_dpp(0, __builtin_bit_cast(int, x), CTRL, 0xF, 0xF, true));
}
DI float add_x32(float v) {
  auto r = __builtin_amdgcn_permlane32_swap(__float_as_uint(v), __float_as_uint(v), false, false);
  return __uint_as_float(r[0]) + __uint_as_float(r[1]);
}
DI float max_x32(float v) {
  auto r = __builtin_amdgcn_permlane32_swap(__float_as_uint(v), __float_as_uint(v), false, false);
  return fmaxf(__uint_as_float(r[0]), __uint_as_float(r[1]));
}
DI float add_x16(float v) {
  auto r = __builtin_amdgcn_permlane16_swap(__float_as_uint(v), __float_as_uint(v), false, false);
  return __uint_as_float(r[0]) + __uint_as_float(r[1]);
}
DI float max_x16(float v) {
  auto r = __builtin_amdgcn_permlane16_swap(__float_as_uint(v), __float_as_uint(v), false, false);
  return fmaxf(__uint_as_float(r[0]), __uint_as_float(r[1]));
}
DI float add_8(float v) { v += dpp_f<0xB1>(v); v += dpp_f<0x4E>(v); v += dpp_f<0x141>(v); return v; }
DI float add_16(float v) { v = add_8(v); v += dpp_f<0x140>(v); return v; }
DI float add_64(float v) { return add_x32(add_x16(add_16(v))); }
DI float max_16(float v) {
  v = fmaxf(v, dpp_f<0xB1>(v)); v = fmaxf(v, dpp_f<0x4E>(v)); v = fmaxf(v, dpp_f<0x141>(v)); v = fmaxf(v, dpp_f<0x140>(v));
  return v;
}
DI float max_32(float v) { return max_x16(max_16(v)); }
DI float max_64(float v) { return max_x32(max_32(v)); }

DI void exp_pack(const f32x16& s, bf16x8& p0, bf16x8& p1) {
  u32x4 a, c;
#pragma unroll
  for (int e = 0; e < 4; e++) {
    a[e] = pack_bf16(__builtin_amdgcn_exp2f(s[2 * e]), __builtin_amdgcn_exp2f(s[2 * e + 1]));
    c[e] = pack_bf16(__builtin_amdgcn_exp2f(s[8 + 2 * e]), __builtin_amdgcn_exp2f(s[8 + 2 * e + 1]));
  }
  p0 = __builtin_bit_cast(bf16x8, a);
  p1 = __builtin_bit_cast(bf16x8, c);
}
#define LD8(p) (*(const bf16x8*)(p))
#define SB() __builtin_amdgcn_sched_barrier(0)
#define EX2(x) __builtin_amdgcn_exp2f(x)

template <int NS>
DI void attn_seg(const char* ka, const char* va, int vmask, const bf16x8 qf, const f32x16& cinit, f32x16& o) {
  constexpr int NT = 2 * NS;
  bf16x8 kn = LD8(ka + 1024);
  f32x16 s = MFMA32(LD8(ka), qf, cinit);
  bf16x8 v0 = LD8(va), v1 = LD8(va + 512);
  bf16x8 vprev, pprev;
#pragma unroll
  for (int j = 0; j < NT; j++) {
    if ((j & 1) == 0) {
      if (NS - 1 - (j >> 1) == 3) __builtin_amdgcn_s_setprio(3);
      else if (NS - 1 - (j >> 1) == 2) __builtin_amdgcn_s_setprio(2);
      else if (NS - 1 - (j >> 1) == 1) __builtin_amdgcn_s_setprio(1);
      else __builtin_amdgcn_s_setprio(0);
    }
    const char* vp = va + (vmask & (((j + 1) >> 1) * 16384)) + ((j + 1) & 1) * 1024;
    const char* kp = ka + ((j + 2) >> 1) * 16384 + ((j + 2) & 1) * 1024;
    float e0[8], e1[8];
    u32x4 pa, pc;
    f32x16 sNext;
    bf16x8 w0, w1, k2;
    SB();
#pragma unroll
    for (int e = 0; e < 4; e++) e0[e] = EX2(s[e]);
    SB();
    if (j > 0) o = MFMA32(vprev, pprev, o);
    SB();
#pragma unroll
    for (int e = 4; e < 8; e++) e0[e] = EX2(s[e]);
#pragma unroll
    for (int e = 0; e < 4; e++) pa[e] = pack_bf16(e0[2 * e], e0[2 * e + 1]);
    SB();
    if (j + 1 < NT) { sNext = MFMA32(kn, qf, cinit); w0 = LD8(vp); w1 = LD8(vp + 512); }
    if (j + 2 < NT) k2 = LD8(kp);
    SB();
#pragma unroll
    for (int e = 0; e < 5; e++) e1[e] = EX2(s[8 + e]);
    SB();
    o = MFMA32(v0, __builtin_bit_cast(bf16x8, pa), o);
    SB();
#pragma unroll
    for (int e = 5; e < 8; e++) e1[e] = EX2(s[8 + e]);
#pragma unroll
    for (int e = 0; e < 4; e++) pc[e] = pack_bf16(e1[2 * e], e1[2 * e + 1]);
    pprev = __builtin_bit_cast(bf16x8, pc); vprev = v1;
    if (j + 1 < NT) { s = sNext; v0 = w0; v1 = w1; }
    if (j + 2 < NT) kn = k2;
  }
  SB();
  o = MFMA32(vprev, pprev, o);
}

typedef __amdgpu_buffer_rsrc_t rsrc_t;
DI void st16(rsrc_t r, unsigned off, u32x4 v) { __builtin_amdgcn_raw_buffer_store_b128(v, r, off, 0, 16); }
DI void st8(rsrc_t r, unsigned off, u32x2 v) { __builtin_amdgcn_raw_buffer_store_b64(v, r, off, 0, 16); }
DI void st4(rsrc_t r, unsigned off, unsigned v) { __builtin_amdgcn_raw_buffer_store_b32(v, r, off, 0, 16); }
DI u32x4 ld16(rsrc_t r, unsigned off) { asm volatile("" : "+v"(off) :: "memory"); return __builtin_amdgcn_raw_buffer_load_b128(r, off, 0, 16); }
DI u32x2 ld8(rsrc_t r, unsigned off) { asm volatile("" : "+v"(off) :: "memory"); return __builtin_amdgcn_raw_buffer_load_b64(r, off, 0, 16); }
DI unsigned ld4(rsrc_t r, unsigned off) { asm volatile("" : "+v"(off) :: "memory"); return __builtin_amdgcn_raw_buffer_load_b32(r, off, 0, 16); }
#define MEMFENCE() asm volatile("" ::: "memory")
constexpr int SPIN_MAX = 100000;
#define SPIN_PAUSE() do { __builtin_amdgcn_s_sleep(2); MEMFENCE(); } while (0)

constexpr int L_RING = 0;
constexpr int P1_XB = 128 * 64 * 2, P1_WB = 64 * 96 * 2;
constexpr int L_P1W = 5 * P1_XB;
constexpr int L_RED = 65536;
constexpr int L_DUMMY = 131072;
constexpr int L_SC = 135168;
constexpr int L_ONES = 151552;
constexpr int L_Q = L_ONES + 2048;
constexpr int L_MISC = L_Q + 8192;
constexpr int L_P1S = L_MISC + 1024;
constexpr int L_TOTAL = L_P1S + 1024;

__global__ __launch_bounds__(1024) void fused_block(const unsigned short* __restrict__ x, const unsigned short* __restrict__ wqkv,
                                                    const unsigned short* __restrict__ wo, const unsigned short* __restrict__ wr,
                                                    const unsigned short* __restrict__ wg, const unsigned short* __restrict__ wu,
                                                    const unsigned short* __restrict__ wd, char* ws,
                                                    unsigned short* __restrict__ out) {
  extern __shared__ __attribute__((aligned(16))) char smem[];
  const int tid = threadIdx.x, lane = tid & 63;
  const int wv = __builtin_amdgcn_readfirstlane(tid >> 6);
  const int b = blockIdx.x, xcd = b & 7, bi = b >> 3;
  const int hp = 2 * xcd + (bi & 1), tb = bi >> 1;
  const rsrc_t rs = __builtin_amdgcn_make_buffer_rsrc(ws, 0, WS_END, 0x00020000);
  const int r = lane & 31, h = lane >> 5;
  const unsigned ONES = 0x3f803f80u;
  float* misc = (float*)(smem + L_MISC);
  const unsigned lbase = __builtin_amdgcn_readfirstlane((unsigned)(size_t)smem);
  ST_DECL ST(0);
  unsigned tag;
  asm volatile("s_load_dword %0, %1, 0x0" : "=s"(tag) : "s"(ws + WS_EP) : "memory");

  {
    const int m0 = tb * 128;
    const int m = wv & 3;
    float* csl = misc + 160;
    float* str = (float*)(smem + L_P1S);
    float* stm = str + 128;
    const unsigned short* xsrc;
    const unsigned short* wsrc;
    {
      const int row = 8 * wv + (lane >> 3);
      xsrc = x + (size_t)(m0 + row) * D_ + ((lane & 7) ^ ((row >> 1) & 7)) * 8;
      const int l48 = lane < 48 ? lane : 0;
      const int kr = l48 / 12, sq = l48 - kr * 12;
      wsrc = wqkv + (size_t)(4 * wv + kr) * (3 * D_) + (sq >> 2) * D_ + hp * 32 + (sq & 3) * 8;
    }
    auto stage = [&](int c) {
      glds16(xsrc + c * 64, lbase + (c % 5) * P1_XB + wv * 1024);
      if (lane < 48) glds16(wsrc + (size_t)c * 64 * (3 * D_), lbase + L_P1W + (c % 5) * P1_WB + wv * 768);
    };
    auto chunk_end = [&](int c) {
      if (c <= 3) WAIT_VM(4); else if (c == 4) WAIT_VM(2); else WAIT_VM(0);
      BAR();
      if (c + 5 < 8) stage(c + 5);
    };
    stage(0); BAR(); stage(1); BAR(); stage(2); stage(3); stage(4);
    if (wv == 15) {
      const unsigned o1v = ((lane & 15) == 0 || (lane & 15) == 4) ? ONES : 0u;
      *(u32x4*)(smem + L_ONES + lane * 16) = (u32x4){o1v, o1v, o1v, o1v};
      *(u32x4*)(smem + L_ONES + 1024 + lane * 16) = (u32x4){o1v, o1v, o1v, o1v};
      ((int*)misc)[128 + lane] = 0; ((int*)misc)[192 + lane] = 0;
    }
    const int q = (lane & 15) >> 2, p = lane & 3;
    WAIT_VM(6);
    RAW_BAR();
    ST(1);
    if (wv < 4) {
      const int xsw = (r >> 1) & 7;
      const int blk = (lane >> 4) & 1;
      unsigned wb = lbase + L_P1W + (8 * h + q) * 192 + blk * 32 + p * 8;
      asm volatile("" : "+v"(wb));
      unsigned xb[2][4];
#pragma unroll
      for (int ks = 0; ks < 4; ks++) {
        xb[0][ks] = lbase + (32 * m + r) * 128 + (((2 * ks + h) ^ xsw) << 4);
        xb[1][ks] = xb[0][ks] + 65536;
        asm volatile("" : "+v"(xb[0][ks]), "+v"(xb[1][ks]));
      }
      auto ldx = [&](int st) {
        const int off = ((st >> 2) % 5) * P1_XB;
        return *(const LDS_AS bf16x8*)(size_t)(xb[off >= 65536][st & 3] + (off & 65535));
      };
      auto ldw = [&](int st, int sg) {
        const unsigned pw = wb + ((st >> 2) % 5) * P1_WB + (st & 3) * 16 * 192 + sg * 64;
        return cat8(__builtin_amdgcn_ds_read_tr16_b64_v4i16((LDS_AS s16x4*)(size_t)pw),
                    __builtin_amdgcn_ds_read_tr16_b64_v4i16((LDS_AS s16x4*)(size_t)(pw + 4 * 192)));
      };
      f32x16 aq, ak, av;
#pragma unroll
      for (int i = 0; i < 16; i++) { aq[i] = 0.f; ak[i] = 0.f; av[i] = 0.f; }
      bf16x8 fx[3], fq[3], fk[3], fv[3];
#pragma unroll
      for (int st = 0; st < 2; st++) { fx[st] = ldx(st); fq[st] = ldw(st, 0); fk[st] = ldw(st, 1); fv[st] = ldw(st, 2); }
#pragma unroll
      for (int st = 0; st < 32; st++) {
        const int cu = st % 3, nx = (st + 2) % 3;
        SB();
        aq = MFMA32(fq[cu], fx[cu], aq);
        if (st + 2 < 32) { fx[nx] = ldx(st + 2); fq[nx] = ldw(st + 2, 0); }
        SB();
        ak = MFMA32(fk[cu], fx[cu], ak);
        if (st + 2 < 32) fk[nx] = ldw(st + 2, 1);
        SB();
        av = MFMA32(fx[cu], fv[cu], av);
        if (st + 2 < 32) fv[nx] = ldw(st + 2, 2);
        SB();
        if ((st & 3) == 3) STP(4 + (st >> 2));
        if ((st & 3) == 3 && st < 24) chunk_end(st >> 2);
      }
      ST(2);
      BAR();
      STP(12);
      const int T = tb * 4 + m;
      const float mean = stm[32 * m + r];
      const float rsd = str[32 * m + r];
      const float rq = rsd * (0.25f * 1.4426950408889634f);
      const float cv = csl[64 + r];
      float vq[16], vk[16], vv[16];
      f32x4 cq4[4], ck4[4], a44[4], b44[4];
#pragma unroll
      for (int g = 0; g < 4; g++) {
        cq4[g] = *(const f32x4*)(csl + 8 * g + 4 * h);
        ck4[g] = *(const f32x4*)(csl + 32 + 8 * g + 4 * h);
        a44[g] = *(const f32x4*)(str + 32 * m + 8 * g + 4 * h);
        b44[g] = *(const f32x4*)(stm + 32 * m + 8 * g + 4 * h);
      }
      SB();
#pragma unroll
      for (int g = 0; g < 4; g++) {
        const f32x4 cq = cq4[g], ck = ck4[g], a4 = a44[g], b4 = b44[g];
#pragma unroll
        for (int e = 0; e < 4; e++) {
          vk[4 * g + e] = (ak[4 * g + e] - mean * ck[e]) * rsd;
          vv[4 * g + e] = (av[4 * g + e] - b4[e] * cv) * a4[e];
          vq[4 * g + e] = (aq[4 * g + e] - mean * cq[e]) * rq;
        }
      }
      float n2[2];
#pragma unroll
      for (int hh = 0; hh < 2; hh++) {
        u32x4 pk;
        n2[hh] = 0.f;
#pragma unroll
        for (int e = 0; e < 4; e++) {
          pk[e] = pack_bf16(vk[8 * hh + 2 * e], vk[8 * hh + 2 * e + 1]);
          n2[hh] = dot2bf(pk[e], pk[e], n2[hh]);
        }
        st16(rs, WS_K + ((2 * hp + hh) * 64 + T) * 1024 + lane * 16, pk);
        *(u32x4*)(smem + L_RING + hh * 8192 + m * 1024 + lane * 16) = pk;
      }
      {
        const int hh = r >> 4, d = r & 15;
#pragma unroll
        for (int s = 0; s < 2; s++) {
          u32x4 pv;
#pragma unroll
          for (int e = 0; e < 4; e++) pv[e] = pack_bf16(vv[8 * s + 2 * e], vv[8 * s + 2 * e + 1]);
          st16(rs, WS_V + ((2 * hp + hh) * 64 + T) * 1024 + s * 512 + h * 256 + d * 16, pv);
          *(u32x4*)(smem + L_RING + hh * 8192 + 4096 + m * 1024 + s * 512 + h * 256 + d * 16) = pv;
        }
      }
#pragma unroll
      for (int hh = 0; hh < 2; hh++) {
        u32x4 pq;
#pragma unroll
        for (int e = 0; e < 4; e++) pq[e] = pack_bf16(vq[8 * hh + 2 * e], vq[8 * hh + 2 * e + 1]);
        *(u32x4*)(smem + L_Q + (hh * 4 + m) * 1024 + lane * 16) = pq;
        const float nm = max_32(add_x32(n2[hh]));
        if (lane == 0) misc[hh * 4 + m] = nm;
      }
      STP(13);
      RAW_BAR();
    } else if (wv < 8) {
      const int g = lane >> 4, rl = lane & 15;
      bf16x8 onesf;
#pragma unroll
      for (int i = 0; i < 8; i++) onesf[i] = 0x3f80;
      f32x4 gq[2], gs[2];
#pragma unroll
      for (int i = 0; i < 2; i++) { gq[i] = (f32x4){0.f, 0.f, 0.f, 0.f}; gs[i] = (f32x4){0.f, 0.f, 0.f, 0.f}; }
      unsigned xa[2][2];
#pragma unroll
      for (int i = 0; i < 2; i++)
#pragma unroll
        for (int kb = 0; kb < 2; kb++) {
          const int row = 32 * m + 16 * i + rl;
          xa[i][kb] = lbase + row * 128 + (((4 * kb + g) ^ ((row >> 1) & 7)) << 4);
        }
      auto stats = [&](int c) {
        bf16x8 f[2][2];
#pragma unroll
        for (int i = 0; i < 2; i++)
#pragma unroll
          for (int kb = 0; kb < 2; kb++) f[i][kb] = *(const LDS_AS bf16x8*)(size_t)(xa[i][kb] + (c % 5) * P1_XB);
#pragma unroll
        for (int kb = 0; kb < 2; kb++)
#pragma unroll
          for (int i = 0; i < 2; i++) { gq[i] = MFMA16(f[i][kb], f[i][kb], gq[i]); gs[i] = MFMA16(f[i][kb], onesf, gs[i]); }
      };
      stats(0);
#pragma unroll
      for (int c = 0; c < 7; c++) { stats(c + 1); if (c < 6) chunk_end(c); }
      if (g == (rl >> 2)) {
#pragma unroll
        for (int i = 0; i < 2; i++) {
          const int e = rl & 3;
          const float sq = e == 0 ? gq[i][0] : e == 1 ? gq[i][1] : e == 2 ? gq[i][2] : gq[i][3];
          const float sm = e == 0 ? gs[i][0] : e == 1 ? gs[i][1] : e == 2 ? gs[i][2] : gs[i][3];
          const float mean = sm * (1.f / D_);
          str[32 * m + 16 * i + rl] = rsqrtf(fmaxf(sq * (1.f / D_) - mean * mean, 0.f) + LN_EPS);
          stm[32 * m + 16 * i + rl] = mean;
        }
      }
      RAW_BAR();
      BAR();
    } else if (wv < 12) {
      const int g = lane >> 4;
      bf16x8 onesf;
#pragma unroll
      for (int i = 0; i < 8; i++) onesf[i] = 0x3f80;
      f32x4 cs[3];
#pragma unroll
      for (int i = 0; i < 3; i++) cs[i] = (f32x4){0.f, 0.f, 0.f, 0.f};
      unsigned wa = lbase + L_P1W + ((m & 1) * 32 + 8 * g + q) * 192 + (m >> 1) * 96 + p * 8;
      asm volatile("" : "+v"(wa));
      auto colsum = [&](int c) {
#pragma unroll
        for (int i = 0; i < 3; i++) {
          const unsigned pw = wa + (c % 5) * P1_WB + i * 32;
          const bf16x8 wf = cat8(__builtin_amdgcn_ds_read_tr16_b64_v4i16((LDS_AS s16x4*)(size_t)pw),
                                 __builtin_amdgcn_ds_read_tr16_b64_v4i16((LDS_AS s16x4*)(size_t)(pw + 4 * 192)));
          cs[i] = MFMA16(onesf, wf, cs[i]);
        }
      };
      colsum(0);
#pragma unroll
      for (int c = 0; c < 7; c++) { colsum(c + 1); if (c < 6) chunk_end(c); }
      if (lane < 16) {
#pragma unroll
        for (int i = 0; i < 3; i++) atomicAdd(csl + (m >> 1) * 48 + i * 16 + lane, cs[i][0]);
      }
      RAW_BAR();
      BAR();
    } else {
#pragma unroll
      for (int c = 0; c < 6; c++) chunk_end(c);
      BAR();
      BAR();
    }
  }
  asm volatile("s_waitcnt lgkmcnt(0)" : "+s"(tag) :: "memory");
  tag += 1u;
  ST(3);

  u32x4 xres, wrv, wr4;
  const int rb = tb * 2 + (hp & 1), ct = hp >> 1;
  const bool is_up = bi < 28;
  const int cgp = xcd * 28 + bi;
  const int dgp = xcd * 4 + (bi - 28);
  const int tok = (tid >> 3) & 63, cg = tid & 7;
  {
    const int a = wv >> 3, kh = (wv >> 2) & 1, qt = wv & 3;
    const int head = 2 * hp + a;
    const bf16x8 qf = LD8(smem + L_Q + (a * 4 + qt) * 1024 + lane * 16);
    float q2;
    {
      u32x4 qu = __builtin_bit_cast(u32x4, qf);
      q2 = dot2bf(qu[0], qu[0], 0.f);
      q2 = dot2bf(qu[1], qu[1], q2); q2 = dot2bf(qu[2], qu[2], q2); q2 = dot2bf(qu[3], qu[3], q2);
      q2 = add_x32(q2);
    }
    const f32x4 kl = *(const f32x4*)(misc + a * 4);
    const float m0row = __builtin_amdgcn_sqrtf(q2 * fmaxf(fmaxf(kl[0], kl[1]), fmaxf(kl[2], kl[3]))) * 1.001f;
    const int kbase = L_RING + a * 8192 + kh * 2048 + lane * 16;
    const int vl = (lane >> 5) * 256 + (lane & 15) * 16;
    const bool ones_lane = (lane & 16) != 0;
    const int vbase = ones_lane ? (L_ONES + vl) : (L_RING + a * 8192 + 4096 + kh * 2048 + vl);
    const int vmask = ones_lane ? 0 : -1;
    f32x16 cinit, o;
#pragma unroll
    for (int i = 0; i < 16; i++) { cinit[i] = -m0row; o[i] = 0.f; }
    attn_seg<1>(smem + kbase, smem + vbase, vmask, qf, cinit, o);
    if (wv < 4) {
      WAIT_VM(0);
      if (lane == 0) {
        const unsigned k0 = (__float_as_uint(misc[wv]) + 0xffffu) >> 16, k1 = (__float_as_uint(misc[4 + wv]) + 0xffffu) >> 16;
        st8(rs, WS_FA + (hp * 64 + tb * 4 + wv) * 8, (u32x2){k0 | (k1 << 16), tag});
      }
    } else if (wv == 4) {
      u32x2 f;
#pragma unroll 1
      for (int it = 0; it < SPIN_MAX; it++) {
        f = ld8(rs, WS_FA + (hp * 64 + lane) * 8);
        if (__all(f[1] == tag)) break;
        SPIN_PAUSE();
      }
      const float k0 = max_64(bf_lo(f[0])), k1 = max_64(bf_hi(f[0]));
      if (lane == 0) { misc[8] = k0; misc[9] = k1; }
    }
    RAW_BAR();
    STL(4);
    const char* gsrc = ws + (((wv >> 2) & 1) ? WS_V : WS_K) + (size_t)(head * 64 + (wv & 3)) * 1024 + lane * 16;
    auto fill = [&](int s) {
      glds16_sc1(gsrc + (size_t)((tb + s) & 15) * 4096, lbase + L_RING + (s & 7) * 16384 + wv * 1024);
    };
#pragma unroll
    for (int s = 1; s < 8; s++) { fill(s); if (s == 3) BAR(); }
    {
      const float m1row = fmaxf(__builtin_amdgcn_sqrtf(q2 * misc[8 + a]) * 1.001f, m0row);
      const float f = EX2(m0row - m1row);
#pragma unroll
      for (int i = 0; i < 9; i++) o[i] *= f;
#pragma unroll
      for (int i = 0; i < 16; i++) cinit[i] = -m1row;
    }
    WAIT_VM(4);
    RAW_BAR();
    STL(5);
    attn_seg<3>(smem + kbase + 16384, smem + vbase + (vmask & 16384), vmask, qf, cinit, o);
#pragma unroll 1
    for (int u = 1; u < 4; u++) {
      if (u < 3) WAIT_VM(0); else WAIT_VM(2);
      RAW_BAR();
      if (u < 3) {
        fill(4 * u + 4); fill(4 * u + 5); fill(4 * u + 6); fill(4 * u + 7);
        if (u == 2) {
          int l2 = lane;
          asm volatile("" : "+v"(l2));
          const int slot = wv * 64 + l2;
          const unsigned short* pa = is_up ? ((slot & 1) ? wu : wg) + (size_t)(slot >> 1) * F_ + cgp * 8 : wr + l2 * 8;
          glds16(pa, lbase + L_DUMMY);
          const int pc = bi * 7 + wv, sl = pc / 56, ds = (pc - sl * 56) * 64 + l2;
          const unsigned short* pb = wv < 7 ? wd + (size_t)(ds >> 1) * D_ + (xcd * 4 + sl) * 16 + (ds & 1) * 8 : wr + l2 * 8;
          glds16(pb, lbase + L_DUMMY);
        }
      } else {
        int l2 = lane;
        asm volatile("" : "+v"(l2));
        const int rr = l2 >> 3;
        const int c = (l2 & 7) ^ (((rr >> 1) & 1) << 2);
        const unsigned short* src = wo + (size_t)(32 * wv + rr) * D_ + (hp >> 1) * 64 + c * 8;
#pragma unroll
        for (int i = 0; i < 4; i++) glds16(src + (size_t)i * 8 * D_, lbase + L_RING + (4 * wv + i) * 1024);
      }
      const int so = (u & 1) * 65536;
      attn_seg<4>(smem + kbase + so, smem + vbase + (vmask & so), vmask, qf, cinit, o);
    }
    __builtin_amdgcn_s_setprio(0);
    STL(6);
    float* lx = (float*)(smem + L_Q);
    float* mx = (float*)(smem + L_Q + 4096);
    float* sc = (float*)(smem + L_SC) + (a * 4 + qt) * 512;
    const int pw = wv ^ 4;
    lx[wv * 64 + lane] = o[8];
    if (kh == 1) {
#pragma unroll
      for (int i = 0; i < 8; i++) sc[i * 64 + lane] = o[i];
    }
    WAIT_VM(0);
    RAW_BAR();
    float ltot = o[8] + lx[pw * 64 + lane];
    const bool bad = !(ltot >= 9.094947e-13f);
    if (__any(bad)) {
      volatile LDS_AS int* psync = (volatile LDS_AS int*)(size_t)((unsigned)(size_t)smem + L_MISC + 512);
      auto pair_sync = [&](int phase) {
        asm volatile("s_waitcnt lgkmcnt(0)" ::: "memory");
        if (lane == 0) psync[wv] = phase;
        while (psync[pw] < phase) __builtin_amdgcn_s_sleep(1);
        MEMFENCE();
      };
      const unsigned gk = WS_K + (head * 64 + kh * 32) * 1024 + lane * 16;
      const unsigned gv = WS_V + (head * 64 + kh * 32) * 1024 + vl;
      float mt = -INFINITY;
      f32x16 zero;
#pragma unroll
      for (int i = 0; i < 16; i++) zero[i] = 0.f;
#pragma unroll 1
      for (int t = 0; t < 32; t++) {
        f32x16 sv = MFMA32(__builtin_bit_cast(bf16x8, ld16(rs, gk + t * 1024)), qf, zero);
#pragma unroll
        for (int i = 0; i < 16; i++) mt = fmaxf(mt, sv[i]);
      }
      mt = max_x32(mt);
      mx[wv * 64 + lane] = mt;
      pair_sync(1);
      mt = fmaxf(mt, mx[pw * 64 + lane]);
      const bf16x8 cst = LD8(smem + L_ONES + vl);
#pragma unroll
      for (int i = 0; i < 16; i++) { cinit[i] = -mt; o[i] = 0.f; }
#pragma unroll 1
      for (int t = 0; t < 32; t++) {
        f32x16 sv = MFMA32(__builtin_bit_cast(bf16x8, ld16(rs, gk + t * 1024)), qf, cinit);
        bf16x8 w0 = __builtin_bit_cast(bf16x8, ld16(rs, gv + t * 1024)), w1 = __builtin_bit_cast(bf16x8, ld16(rs, gv + t * 1024 + 512));
        if (ones_lane) { w0 = cst; w1 = cst; }
        bf16x8 p0, p1;
        exp_pack(sv, p0, p1);
        o = MFMA32(w0, p0, o);
        o = MFMA32(w1, p1, o);
      }
      lx[wv * 64 + lane] = o[8];
      if (kh == 1) {
#pragma unroll
        for (int i = 0; i < 8; i++) sc[i * 64 + lane] = o[i];
      }
      pair_sync(2);
      ltot = o[8] + lx[pw * 64 + lane];
    }
    if (kh == 0) {
      const float inv = 1.f / ltot;
      u32x4 po;
#pragma unroll
      for (int e = 0; e < 4; e++) {
        const float x0 = (o[2 * e] + sc[(2 * e) * 64 + lane]) * inv;
        const float x1 = (o[2 * e + 1] + sc[(2 * e + 1) * 64 + lane]) * inv;
        po[e] = pack_bf16(x0, x1);
      }
      st16(rs, WS_O + ((tb * 4 + qt) * 32 + head) * 1024 + lane * 16, po);
      WAIT_VM(0);
      if (lane == 0) st4(rs, WS_FB + ((tb * 4 + qt) * 32 + head) * 4, tag);
    }
  }
  STL(7);

  float o1[8];
  int I1 = 0, I2 = 0;
  {
    float* red = (float*)(smem + L_RED);
    if (wv < 8) {
      int t2 = tid;
      asm volatile("" : "+v"(t2));
      xres = *(const u32x4*)(x + (size_t)(rb * 64 + (t2 >> 3)) * D_ + ct * 64 + (t2 & 7) * 8);
      wrv = *(const u32x4*)(wr + ct * 64 + (t2 & 7) * 8);
      wr4 = *(const u32x4*)(wr + (t2 & 63) * 8);
    }
    if (wv < 8) {
      const int m = wv & 1, kq = wv >> 1;
      const int T = rb * 2 + m;
#pragma unroll 1
      for (int it = 0; it < SPIN_MAX; it++) {
        const unsigned f = ld4(rs, WS_FB + (T * 32 + kq * 8 + (lane & 7)) * 4);
        if (__all(f == tag)) break;
        SPIN_PAUSE();
      }
      MEMFENCE();
      STL(8);
      u32x4 of[8];
#pragma unroll
      for (int i = 0; i < 8; i++) of[i] = ld16(rs, WS_O + (T * 32 + kq * 8 + i) * 1024 + lane * 16);
      MEMFENCE();
      const int blk = (lane >> 4) & 1, q = (lane & 15) >> 2, p = lane & 3;
      f32x16 acc[2];
#pragma unroll
      for (int i = 0; i < 16; i++) { acc[0][i] = 0.f; acc[1][i] = 0.f; }
      const int rsw = ((q >> 1) & 1) << 2;
#pragma unroll
      for (int i = 0; i < 8; i++) {
        const char* rowp = smem + L_RING + (kq * 128 + i * 16 + 4 * h + q) * 128;
#pragma unroll
        for (int n = 0; n < 2; n++) {
          const int chunk = (n * 4 + blk * 2 + (p >> 1)) ^ rsw;
          const char* pw = rowp + chunk * 16 + (p & 1) * 8;
          bf16x8 wf = cat8(tr_read(pw), tr_read(pw + 8 * 128));
          acc[n] = MFMA32(wf, __builtin_bit_cast(bf16x8, of[i]), acc[n]);
        }
      }
      float* dst = red + (kq * 64 + m * 32 + r) * 68 + 4 * h;
#pragma unroll
      for (int n = 0; n < 2; n++)
#pragma unroll
        for (int g = 0; g < 4; g++)
          *(f32x4*)(dst + n * 32 + 8 * g) = (f32x4){acc[n][4 * g], acc[n][4 * g + 1], acc[n][4 * g + 2], acc[n][4 * g + 3]};
    }
    RAW_BAR();
    STL(9);
    if (wv >= 8) {
      if (is_up) {
#pragma unroll
        for (int i = 0; i < 2; i++) {
          const int slot = ((wv - 8) * 2 + i) * 64 + lane;
          const unsigned short* src = ((slot & 1) ? wu : wg) + (size_t)(slot >> 1) * F_ + cgp * 8;
          glds16(src, lbase + L_SC + ((wv - 8) * 2 + i) * 1024);
        }
      } else {
#pragma unroll
        for (int i = 0; i < 7; i++) {
          const int slot = ((wv - 8) * 7 + i) * 64 + lane;
          glds16(wd + (size_t)(slot >> 1) * D_ + dgp * 16 + (slot & 1) * 8, lbase + L_RING + ((wv - 8) * 7 + i) * 1024);
        }
      }
    }
    float rv = -INFINITY;
    u32x4 hn4;
    if (wv < 8) {
      f32x4 a0 = {0.f, 0.f, 0.f, 0.f}, a1 = {0.f, 0.f, 0.f, 0.f};
#pragma unroll
      for (int k = 0; k < 4; k++) {
        const float* src = red + (k * 64 + tok) * 68 + cg * 8;
        a0 += *(const f32x4*)src;
        a1 += *(const f32x4*)(src + 4);
      }
      u32x4 hq;
      hq[0] = pack_bf16(a0[0] + bf_lo(xres[0]), a0[1] + bf_hi(xres[0]));
      hq[1] = pack_bf16(a0[2] + bf_lo(xres[1]), a0[3] + bf_hi(xres[1]));
      hq[2] = pack_bf16(a1[0] + bf_lo(xres[2]), a1[1] + bf_hi(xres[2]));
      hq[3] = pack_bf16(a1[2] + bf_lo(xres[3]), a1[3] + bf_hi(xres[3]));
      float s1 = 0.f, s2 = 0.f, s3 = 0.f;
#pragma unroll
      for (int e = 0; e < 4; e++) {
        s1 = dot2bf(hq[e], ONES, s1);
        s2 = dot2bf(hq[e], hq[e], s2);
        s3 = dot2bf(hq[e], wrv[e], s3);
      }
      s1 = add_8(s1); s2 = add_8(s2); s3 = add_8(s3);
      if (cg == 0) st16(rs, WS_C1 + ((rb * 8 + ct) * 64 + tok) * 16, (u32x4){__float_as_uint(s1), __float_as_uint(s2), __float_as_uint(s3), tag});
      float wsum;
      {
        const u32x4 w4 = wr4;
        wsum = dot2bf(w4[0], ONES, 0.f); wsum = dot2bf(w4[1], ONES, wsum);
        wsum = dot2bf(w4[2], ONES, wsum); wsum = dot2bf(w4[3], ONES, wsum);
        wsum = add_64(wsum);
      }
      u32x4 g4;
#pragma unroll 1
      for (int it = 0; it < SPIN_MAX; it++) {
        g4 = ld16(rs, WS_C1 + ((rb * 8 + cg) * 64 + tok) * 16);
        if (__all(g4[3] == tag)) break;
        SPIN_PAUSE();
      }
      STL(10);
      const float t1 = add_8(__uint_as_float(g4[0])), t2 = add_8(__uint_as_float(g4[1])), t3 = add_8(__uint_as_float(g4[2]));
      const float mean = t1 * (1.f / D_);
      const float rstd = rsqrtf(fmaxf(t2 * (1.f / D_) - mean * mean, 0.f) + LN_EPS);
      rv = rstd * (t3 - mean * wsum);
#pragma unroll
      for (int e = 0; e < 4; e++) {
        const float h0 = bf_lo(hq[e]), h1 = bf_hi(hq[e]);
        const float n0 = (h0 - mean) * rstd, n1 = (h1 - mean) * rstd;
        hn4[e] = pack_bf16(n0, n1);
        o1[2 * e] = h0 + n0; o1[2 * e + 1] = h1 + n1;
      }
      const float M1 = max_64(rv);
      unsigned long long bm = __ballot(rv == M1);
      const int t1i = __builtin_amdgcn_readlane(tok, bm ? __builtin_ctzll(bm) : 0);
      const float c2 = (tok == t1i) ? -INFINITY : rv;
      const float M2 = max_64(c2);
      bm = __ballot(c2 == M2 && tok != t1i);
      const int t2i = __builtin_amdgcn_readlane(tok, bm ? __builtin_ctzll(bm) : 0);
      if (lane == 0) *(f32x4*)(misc + 16 + wv * 4) = (f32x4){M1, __int_as_float(t1i), M2, __int_as_float(t2i)};
    }
    RAW_BAR();
    if (wv < 8) {
      const float cv = misc[16 + ((lane & 15) >> 1) * 4 + (lane & 1) * 2];
      const int ci = __float_as_int(misc[16 + ((lane & 15) >> 1) * 4 + (lane & 1) * 2 + 1]);
      const float M1 = max_16(cv);
      unsigned long long bm = __ballot(cv == M1);
      I1 = __builtin_amdgcn_readlane(ci, bm ? __builtin_ctzll(bm) : 0);
      const float c2 = (ci == I1) ? -INFINITY : cv;
      const float M2 = max_16(c2);
      bm = __ballot(c2 == M2 && ci != I1);
      I2 = __builtin_amdgcn_readlane(ci, bm ? __builtin_ctzll(bm) : 0);
      if (I2 == I1) I2 = I1 ^ 1;
      if (tok == I1 || tok == I2) {
        const int cd = rb * 2 + (tok == I2 ? 1 : 0);
        const unsigned xo = WS_XS + (cd * 256 + ct * 32 + cg * 4) * 8;
        st16(rs, xo, (u32x4){hn4[0], tag, hn4[1], tag});
        st16(rs, xo + 16, (u32x4){hn4[2], tag, hn4[3], tag});
        const unsigned oo = WS_O1 + (cd * 512 + ct * 64 + cg * 8) * 8;
#pragma unroll
        for (int e = 0; e < 4; e++)
          st16(rs, oo + e * 16, (u32x4){__float_as_uint(o1[2 * e]), tag, __float_as_uint(o1[2 * e + 1]), tag});
        if (ct == 0 && cg == 0) st16(rs, WS_C2 + cd * 16, (u32x4){__float_as_uint(rv), (unsigned)(rb * 64 + tok), 0u, tag});
      } else {
        u32x4 ov;
#pragma unroll
        for (int e = 0; e < 4; e++) ov[e] = pack_bf16(o1[2 * e], o1[2 * e + 1]);
        *(u32x4*)(out + (size_t)(rb * 64 + tok) * D_ + ct * 64 + cg * 8) = ov;
      }
    } else if (wv == 8) {
      u32x4 g4;
#pragma unroll 1
      for (int it = 0; it < SPIN_MAX; it++) {
        g4 = ld16(rs, WS_C2 + lane * 16);
        if (__all(g4[3] == tag)) break;
        SPIN_PAUSE();
      }
      const float cv = __uint_as_float(g4[0]);
      const float M1 = max_64(cv);
      unsigned long long bm = __ballot(cv == M1);
      const int l1 = bm ? __builtin_ctzll(bm) : 0;
      const float c2 = (lane == l1) ? -INFINITY : cv;
      const float M2 = max_64(c2);
      bm = __ballot(c2 == M2 && lane != l1);
      const int l2 = bm ? __builtin_ctzll(bm) : (l1 ^ 1);
      const int r1 = __builtin_amdgcn_readlane((int)g4[1], l1), r2 = __builtin_amdgcn_readlane((int)g4[1], l2);
      if (lane == 0) {
        *(f32x4*)(misc + 64) = (f32x4){__int_as_float(l1), __int_as_float(l2), __int_as_float(r1 & (S_ - 1)), __int_as_float(r2 & (S_ - 1))};
        misc[68] = M1; misc[69] = M2;
        if (b == 0) st4(rs, WS_EP, tag);
      }
    }
    STL(11);
    if (wv >= 8) WAIT_VM(0);
    RAW_BAR();
    STL(12);
  }
  const int cdA = __float_as_int(misc[64]), cdB = __float_as_int(misc[65]);
  const int rowA = __float_as_int(misc[66]), rowB = __float_as_int(misc[67]);
  if (wv < 8 && (tok == I1 || tok == I2)) {
    const int grow = rb * 64 + tok;
    if (grow != rowA && grow != rowB) {
      u32x4 ov;
#pragma unroll
      for (int e = 0; e < 4; e++) ov[e] = pack_bf16(o1[2 * e], o1[2 * e + 1]);
      *(u32x4*)(out + (size_t)grow * D_ + ct * 64 + cg * 8) = ov;
    }
  }
  if (is_up) {
    char* xs = smem + L_RED;
    if (wv >= 8 && wv < 12) {
      const int t = tid - 512, k = t >> 7, g2 = t & 127;
      const unsigned off = WS_XS + ((k ? cdB : cdA) * 256 + g2 * 2) * 8;
      u32x4 g4;
#pragma unroll 1
      for (int it = 0; it < SPIN_MAX; it++) {
        g4 = ld16(rs, off);
        if (__all(g4[1] == tag && g4[3] == tag)) break;
        SPIN_PAUSE();
      }
      *(u32x2*)(xs + k * 1024 + g2 * 8) = (u32x2){g4[0], g4[2]};
    }
    RAW_BAR();
    STL(13);
    if (wv == 8) {
      const int g = lane >> 4, q = (lane & 15) >> 2, p = lane & 3;
      const char* xa = xs + (lane & 1) * 1024 + g * 16;
      const char* wa = smem + L_SC + (8 * g + q) * 32 + p * 8;
      f32x4 c0 = {0.f, 0.f, 0.f, 0.f}, c1 = {0.f, 0.f, 0.f, 0.f};
#pragma unroll
      for (int s = 0; s < 16; s += 2) {
        c0 = MFMA16(LD8(xa + s * 64), cat8(tr_read(wa + s * 1024), tr_read(wa + s * 1024 + 128)), c0);
        c1 = MFMA16(LD8(xa + s * 64 + 64), cat8(tr_read(wa + s * 1024 + 1024), tr_read(wa + s * 1024 + 1152)), c1);
      }
      const float g0 = c0[0] + c1[0], g1 = c0[1] + c1[1];
      const float u0 = dpp_f<0x128>(g0), u1 = dpp_f<0x128>(g1);
      if (lane < 8) {
        const unsigned gp = pack_bf16(g0, g1), up = pack_bf16(u0, u1);
        const float ga = bf_lo(gp), gb = bf_hi(gp);
        const unsigned sp = pack_bf16(ga / (1.f + __expf(-ga)), gb / (1.f + __expf(-gb)));
        const unsigned ap = pack_bf16(bf_lo(sp) * bf_lo(up), bf_hi(sp) * bf_hi(up));
        st8(rs, WS_DD + (cgp * 8 + lane) * 8, (u32x2){ap, tag});
      }
    }
  } else {
    unsigned short* act = (unsigned short*)(smem + L_RED);
    float* red2 = (float*)(smem + L_RED + 8192);
    float base = 0.f;
    if (wv == 15 && lane < 32) {
      const unsigned off = WS_O1 + (((lane >> 4) ? cdB : cdA) * 512 + dgp * 16 + (lane & 15)) * 8;
      u32x2 g2;
#pragma unroll 1
      for (int it = 0; it < SPIN_MAX; it++) {
        g2 = ld8(rs, off);
        if (__all(g2[1] == tag)) break;
        SPIN_PAUSE();
      }
      base = __uint_as_float(g2[0]);
    }
    if (wv < 14) {
      u32x4 g4;
#pragma unroll 1
      for (int it = 0; it < SPIN_MAX; it++) {
        g4 = ld16(rs, WS_DD + tid * 16);
        if (__all(g4[1] == tag && g4[3] == tag)) break;
        SPIN_PAUSE();
      }
      *(unsigned*)(act + tid * 2) = (g4[0] & 0xffffu) | (g4[2] << 16);
      *(unsigned*)(act + F_ + tid * 2) = (g4[0] >> 16) | (g4[2] & 0xffff0000u);
    }
    RAW_BAR();
    STL(14);
    if (wv < 8) {
      const int g = lane >> 4, q = (lane & 15) >> 2, p = lane & 3;
      const char* xa = (const char*)act + (lane & 1) * (F_ * 2) + g * 16 + wv * 7 * 64;
      const char* wa = smem + L_RING + (wv * 7 * 32 + 8 * g + q) * 32 + p * 8;
      f32x4 c0 = {0.f, 0.f, 0.f, 0.f};
#pragma unroll
      for (int s = 0; s < 7; s++)
        c0 = MFMA16(LD8(xa + s * 64), cat8(tr_read(wa + s * 1024), tr_read(wa + s * 1024 + 128)), c0);
      if (lane < 16) { red2[wv * 32 + lane] = c0[0]; red2[wv * 32 + 16 + lane] = c0[1]; }
    }
    RAW_BAR();
    if (wv == 15 && lane < 32) {
      float y = 0.f;
#pragma unroll
      for (int k = 0; k < 8; k++) y += red2[k * 32 + lane];
      const float rsel = misc[68 + (lane >> 4)];
      const float gate = 1.f / (1.f + __expf(-rsel));
      const int grow = (lane >> 4) ? rowB : rowA;
      out[(size_t)grow * D_ + dgp * 16 + (lane & 15)] = (unsigned short)(pack_bf16(base + y * gate, 0.f) & 0xffffu);
    }
  }
  STL(15);
  ST_FLUSH(0);
}

extern "C" void kernel_launch(void* const* d_in, const int* in_sizes, int n_in,
                              void* d_out, int out_size, void* d_ws, size_t ws_size,
                              hipStream_t stream) {
  (void)hipFuncSetAttribute((const void*)fused_block, hipFuncAttributeMaxDynamicSharedMemorySize, L_TOTAL);
  fused_block<<<256, 1024, L_TOTAL, stream>>>((const unsigned short*)d_in[0], (const unsigned short*)d_in[1],
                                             (const unsigned short*)d_in[2], (const unsigned short*)d_in[4],
                                             (const unsigned short*)d_in[5], (const unsigned short*)d_in[6],
                                             (const unsigned short*)d_in[7], (char*)d_ws, (unsigned short*)d_out);
}
```

```cpp
#include <hip/hip_runtime.h>
#include <hip/hip_bf16.h>
#include <stdint.h>

typedef __attribute__((ext_vector_type(8))) short bf16x8;
typedef __attribute__((ext_vector_type(4))) short s16x4;
typedef __attribute__((ext_vector_type(2))) __bf16 bf16x2_t;
typedef __attribute__((ext_vector_type(16))) float f32x16;
typedef __attribute__((ext_vector_type(4))) float f32x4;
typedef __attribute__((ext_vector_type(2))) float f32x2;
typedef __attribute__((ext_vector_type(4))) unsigned u32x4;
typedef __attribute__((ext_vector_type(2))) unsigned u32x2;

#define DI __device__ __forceinline__
#define LDS_AS __attribute__((address_space(3)))
#define MFMA32(a, b, c) __builtin_amdgcn_mfma_f32_32x32x16_bf16((a), (b), (c), 0, 0, 0)
#define MFMA16(a, b, c) __builtin_amdgcn_mfma_f32_16x16x32_bf16((a), (b), (c), 0, 0, 0)
#define WAIT_VM(n) asm volatile("s_waitcnt vmcnt(" #n ")" ::: "memory")
#define BAR() do { asm volatile("" ::: "memory"); __builtin_amdgcn_s_barrier(); asm volatile("" ::: "memory"); } while (0)
#define RAW_BAR() do { asm volatile("s_waitcnt lgkmcnt(0)" ::: "memory"); __builtin_amdgcn_s_barrier(); asm volatile("" ::: "memory"); } while (0)

#define ST_DECL
#define ST(slot)
#define ST_FLUSH(kid)
#define STP(slot)
#define STL(slot)

constexpr int S_ = 2048, D_ = 512, H_ = 32, F_ = 1792;
constexpr float LN_EPS = 1e-5f;

constexpr unsigned WS_Q = 0;
constexpr unsigned WS_K = WS_Q + S_ * D_ * 2;
constexpr unsigned WS_V = WS_K + S_ * D_ * 2;
constexpr unsigned WS_O = WS_V + S_ * D_ * 2;
constexpr unsigned WS_H = WS_O + S_ * D_ * 2;
constexpr unsigned WS_FA = WS_H + S_ * D_ * 2;
constexpr unsigned WS_FB = WS_FA + 16 * 64 * 8;
constexpr unsigned WS_C1 = WS_FB + 64 * 32 * 4;
constexpr unsigned WS_C2 = WS_C1 + 32 * 8 * 64 * 16;
constexpr unsigned WS_XS = WS_C2 + 8 * 32 * 2 * 16;
constexpr unsigned WS_O1 = WS_XS + 32 * 2 * 256 * 8;
constexpr unsigned WS_DD = WS_O1 + 32 * 2 * 512 * 8;
constexpr unsigned WS_EP = WS_DD + F_ * 8;
constexpr unsigned WS_END = WS_EP + 256;

DI unsigned pack_bf16(float a, float b) {
  f32x2 v = {a, b};
  bf16x2_t r = __builtin_convertvector(v, bf16x2_t);
  return __builtin_bit_cast(unsigned, r);
}
DI float bf_lo(unsigned u) { return __uint_as_float(u << 16); }
DI float bf_hi(unsigned u) { return __uint_as_float(u & 0xffff0000u); }
DI float dot2bf(unsigned a, unsigned b, float c) {
  return __builtin_amdgcn_fdot2_f32_bf16(__builtin_bit_cast(bf16x2_t, a), __builtin_bit_cast(bf16x2_t, b), c, false);
}
DI void glds16(const void* g, unsigned lds_base) {
  unsigned keep;
  asm volatile("s_mov_b32 %0, m0\n\ts_mov_b32 m0, %2\n\ts_nop 0\n\tglobal_load_lds_dwordx4 %1, off\n\ts_mov_b32 m0, %0"
               : "=&s"(keep) : "v"(g), "s"(lds_base) : "memory");
}
DI void glds16_sc1(const void* g, unsigned lds_base) {
  unsigned keep;
  asm volatile("s_mov_b32 %0, m0\n\ts_mov_b32 m0, %2\n\ts_nop 0\n\tglobal_load_lds_dwordx4 %1, off sc1\n\ts_mov_b32 m0, %0"
               : "=&s"(keep) : "v"(g), "s"(lds_base) : "memory");
}
DI s16x4 tr_read(const char* p) {
  return __builtin_amdgcn_ds_read_tr16_b64_v4i16((LDS_AS s16x4*)(size_t)(unsigned)(size_t)p);
}
DI bf16x8 cat8(s16x4 lo, s16x4 hi) { return __builtin_shufflevector(lo, hi, 0, 1, 2, 3, 4, 5, 6, 7); }
template <int CTRL> DI float dpp_f(float x) {
  return __builtin_bit_cast(float, __builtin_amdgcn_update_dpp(0, __builtin_bit_cast(int, x), CTRL, 0xF, 0xF, true));
}
DI float add_x32(float v) {
  auto r = __builtin_amdgcn_permlane32_swap(__float_as_uint(v), __float_as_uint(v), false, false);
  return __uint_as_float(r[0]) + __uint_as_float(r[1]);
}
DI float max_x32(float v) {
  auto r = __builtin_amdgcn_permlane32_swap(__float_as_uint(v), __float_as_uint(v), false, false);
  return fmaxf(__uint_as_float(r[0]), __uint_as_float(r[1]));
}
DI float add_x16(float v) {
  auto r = __builtin_amdgcn_permlane16_swap(__float_as_uint(v), __float_as_uint(v), false, false);
  return __uint_as_float(r[0]) + __uint_as_float(r[1]);
}
DI float max_x16(float v) {
  auto r = __builtin_amdgcn_permlane16_swap(__float_as_uint(v), __float_as_uint(v), false, false);
  return fmaxf(__uint_as_float(r[0]), __uint_as_float(r[1]));
}
DI float add_8(float v) { v += dpp_f<0xB1>(v); v += dpp_f<0x4E>(v); v += dpp_f<0x141>(v); return v; }
DI float add_16(float v) { v = add_8(v); v += dpp_f<0x140>(v); return v; }
DI float add_64(float v) { return add_x32(add_x16(add_16(v))); }
DI float max_16(float v) {
  v = fmaxf(v, dpp_f<0xB1>(v)); v = fmaxf(v, dpp_f<0x4E>(v)); v = fmaxf(v, dpp_f<0x141>(v)); v = fmaxf(v, dpp_f<0x140>(v));
  return v;
}
DI float max_32(float v) { return max_x16(max_16(v)); }
DI float max_64(float v) { return max_x32(max_32(v)); }

DI void exp_pack(const f32x16& s, bf16x8& p0, bf16x8& p1) {
  u32x4 a, c;
#pragma unroll
  for (int e = 0; e < 4; e++) {
    a[e] = pack_bf16(__builtin_amdgcn_exp2f(s[2 * e]), __builtin_amdgcn_exp2f(s[2 * e + 1]));
    c[e] = pack_bf16(__builtin_amdgcn_exp2f(s[8 + 2 * e]), __builtin_amdgcn_exp2f(s[8 + 2 * e + 1]));
  }
  p0 = __builtin_bit_cast(bf16x8, a);
  p1 = __builtin_bit_cast(bf16x8, c);
}
#define LD8(p) (*(const bf16x8*)(p))
#define SB() __builtin_amdgcn_sched_barrier(0)
#define EX2(x) __builtin_amdgcn_exp2f(x)

template <int NS>
DI void attn_seg(const char* ka, const char* va, int vmask, const bf16x8 qf, const f32x16& cinit, f32x16& o) {
  constexpr int NT = 2 * NS;
  bf16x8 kn = LD8(ka + 1024);
  f32x16 s = MFMA32(LD8(ka), qf, cinit);
  bf16x8 v0 = LD8(va), v1 = LD8(va + 512);
  bf16x8 vprev, pprev;
#pragma unroll
  for (int j = 0; j < NT; j++) {
    if ((j & 1) == 0) {
      if (NS - 1 - (j >> 1) == 3) __builtin_amdgcn_s_setprio(3);
      else if (NS - 1 - (j >> 1) == 2) __builtin_amdgcn_s_setprio(2);
      else if (NS - 1 - (j >> 1) == 1) __builtin_amdgcn_s_setprio(1);
      else __builtin_amdgcn_s_setprio(0);
    }
    const char* vp = va + (vmask & (((j + 1) >> 1) * 16384)) + ((j + 1) & 1) * 1024;
    const char* kp = ka + ((j + 2) >> 1) * 16384 + ((j + 2) & 1) * 1024;
    float e0[8], e1[8];
    u32x4 pa, pc;
    f32x16 sNext;
    bf16x8 w0, w1, k2;
    SB();
#pragma unroll
    for (int e = 0; e < 4; e++) e0[e] = EX2(s[e]);
    SB();
    if (j > 0) o = MFMA32(vprev, pprev, o);
    SB();
#pragma unroll
    for (int e = 4; e < 8; e++) e0[e] = EX2(s[e]);
#pragma unroll
    for (int e = 0; e < 4; e++) pa[e] = pack_bf16(e0[2 * e], e0[2 * e + 1]);
    SB();
    if (j + 1 < NT) { sNext = MFMA32(kn, qf, cinit); w0 = LD8(vp); w1 = LD8(vp + 512); }
    if (j + 2 < NT) k2 = LD8(kp);
    SB();
#pragma unroll
    for (int e = 0; e < 5; e++) e1[e] = EX2(s[8 + e]);
    SB();
    o = MFMA32(v0, __builtin_bit_cast(bf16x8, pa), o);
    SB();
#pragma unroll
    for (int e = 5; e < 8; e++) e1[e] = EX2(s[8 + e]);
#pragma unroll
    for (int e = 0; e < 4; e++) pc[e] = pack_bf16(e1[2 * e], e1[2 * e + 1]);
    pprev = __builtin_bit_cast(bf16x8, pc); vprev = v1;
    if (j + 1 < NT) { s = sNext; v0 = w0; v1 = w1; }
    if (j + 2 < NT) kn = k2;
  }
  SB();
  o = MFMA32(vprev, pprev, o);
}

typedef __amdgpu_buffer_rsrc_t rsrc_t;
DI void st16(rsrc_t r, unsigned off, u32x4 v) { __builtin_amdgcn_raw_buffer_store_b128(v, r, off, 0, 16); }
DI void st8(rsrc_t r, unsigned off, u32x2 v) { __builtin_amdgcn_raw_buffer_store_b64(v, r, off, 0, 16); }
DI void st4(rsrc_t r, unsigned off, unsigned v) { __builtin_amdgcn_raw_buffer_store_b32(v, r, off, 0, 16); }
DI u32x4 ld16(rsrc_t r, unsigned off) { asm volatile("" : "+v"(off) :: "memory"); return __builtin_amdgcn_raw_buffer_load_b128(r, off, 0, 16); }
DI u32x2 ld8(rsrc_t r, unsigned off) { asm volatile("" : "+v"(off) :: "memory"); return __builtin_amdgcn_raw_buffer_load_b64(r, off, 0, 16); }
DI unsigned ld4(rsrc_t r, unsigned off) { asm volatile("" : "+v"(off) :: "memory"); return __builtin_amdgcn_raw_buffer_load_b32(r, off, 0, 16); }
#define MEMFENCE() asm volatile("" ::: "memory")
constexpr int SPIN_MAX = 100000;
#define SPIN_PAUSE() do { __builtin_amdgcn_s_sleep(2); MEMFENCE(); } while (0)

constexpr int L_RING = 0;
constexpr int P1_XB = 128 * 64 * 2, P1_WB = 64 * 96 * 2;
constexpr int L_P1W = 5 * P1_XB;
constexpr int L_RED = 65536;
constexpr int L_DUMMY = 131072;
constexpr int L_SC = 135168;
constexpr int L_ONES = 151552;
constexpr int L_Q = L_ONES + 2048;
constexpr int L_MISC = L_Q + 8192;
constexpr int L_P1S = L_MISC + 1024;
constexpr int L_TOTAL = L_P1S + 1024;

__global__ __launch_bounds__(1024) void fused_block(const unsigned short* __restrict__ x, const unsigned short* __restrict__ wqkv,
                                                    const unsigned short* __restrict__ wo, const unsigned short* __restrict__ wr,
                                                    const unsigned short* __restrict__ wg, const unsigned short* __restrict__ wu,
                                                    const unsigned short* __restrict__ wd, char* ws,
                                                    unsigned short* __restrict__ out) {
  extern __shared__ __attribute__((aligned(16))) char smem[];
  const int tid = threadIdx.x, lane = tid & 63;
  const int wv = __builtin_amdgcn_readfirstlane(tid >> 6);
  const int b = blockIdx.x, xcd = b & 7, bi = b >> 3;
  const int hp = 2 * xcd + (bi & 1), tb = bi >> 1;
  const rsrc_t rs = __builtin_amdgcn_make_buffer_rsrc(ws, 0, WS_END, 0x00020000);
  const int r = lane & 31, h = lane >> 5;
  const unsigned ONES = 0x3f803f80u;
  float* misc = (float*)(smem + L_MISC);
  const unsigned lbase = __builtin_amdgcn_readfirstlane((unsigned)(size_t)smem);
  ST_DECL ST(0);
  unsigned tag;
  asm volatile("s_load_dword %0, %1, 0x0" : "=s"(tag) : "s"(ws + WS_EP) : "memory");

  {
    const int m0 = tb * 128;
    const int m = wv & 3;
    float* csl = misc + 160;
    float* str = (float*)(smem + L_P1S);
    float* stm = str + 128;
    const unsigned short* xsrc;
    const unsigned short* wsrc;
    {
      const int row = 8 * wv + (lane >> 3);
      xsrc = x + (size_t)(m0 + row) * D_ + ((lane & 7) ^ ((row >> 1) & 7)) * 8;
      const int l48 = lane < 48 ? lane : 0;
      const int kr = l48 / 12, sq = l48 - kr * 12;
      wsrc = wqkv + (size_t)(4 * wv + kr) * (3 * D_) + (sq >> 2) * D_ + hp * 32 + (sq & 3) * 8;
    }
    auto stage = [&](int c) {
      glds16(xsrc + c * 64, lbase + (c % 5) * P1_XB + wv * 1024);
      if (lane < 48) glds16(wsrc + (size_t)c * 64 * (3 * D_), lbase + L_P1W + (c % 5) * P1_WB + wv * 768);
    };
    auto chunk_end = [&](int c) {
      if (c <= 3) WAIT_VM(4); else if (c == 4) WAIT_VM(2); else WAIT_VM(0);
      BAR();
      if (c + 5 < 8) stage(c + 5);
    };
    stage(0); BAR(); stage(1); BAR(); stage(2); stage(3); stage(4);
    if (wv == 15) {
      const unsigned o1v = ((lane & 15) == 0 || (lane & 15) == 4) ? ONES : 0u;
      *(u32x4*)(smem + L_ONES + lane * 16) = (u32x4){o1v, o1v, o1v, o1v};
      *(u32x4*)(smem + L_ONES + 1024 + lane * 16) = (u32x4){o1v, o1v, o1v, o1v};
      ((int*)misc)[128 + lane] = 0; ((int*)misc)[192 + lane] = 0;
    }
    const int q = (lane & 15) >> 2, p = lane & 3;
    WAIT_VM(6);
    RAW_BAR();
    ST(1);
    if (wv < 4) {
      const int xsw = (r >> 1) & 7;
      const int blk = (lane >> 4) & 1;
      unsigned wb = lbase + L_P1W + (8 * h + q) * 192 + blk * 32 + p * 8;
      asm volatile("" : "+v"(wb));
      unsigned xb[2][4];
#pragma unroll
      for (int ks = 0; ks < 4; ks++) {
        xb[0][ks] = lbase + (32 * m + r) * 128 + (((2 * ks + h) ^ xsw) << 4);
        xb[1][ks] = xb[0][ks] + 65536;
        asm volatile("" : "+v"(xb[0][ks]), "+v"(xb[1][ks]));
      }
      auto ldx = [&](int st) {
        const int off = ((st >> 2) % 5) * P1_XB;
        return *(const LDS_AS bf16x8*)(size_t)(xb[off >= 65536][st & 3] + (off & 65535));
      };
      auto ldw = [&](int st, int sg) {
        const unsigned pw = wb + ((st >> 2) % 5) * P1_WB + (st & 3) * 16 * 192 + sg * 64;
        return cat8(__builtin_amdgcn_ds_read_tr16_b64_v4i16((LDS_AS s16x4*)(size_t)pw),
                    __builtin_amdgcn_ds_read_tr16_b64_v4i16((LDS_AS s16x4*)(size_t)(pw + 4 * 192)));
      };
      f32x16 aq, ak, av;
#pragma unroll
      for (int i = 0; i < 16; i++) { aq[i] = 0.f; ak[i] = 0.f; av[i] = 0.f; }
      bf16x8 fx[3], fq[3], fk[3], fv[3];
#pragma unroll
      for (int st = 0; st < 2; st++) { fx[st] = ldx(st); fq[st] = ldw(st, 0); fk[st] = ldw(st, 1); fv[st] = ldw(st, 2); }
#pragma unroll
      for (int st = 0; st < 32; st++) {
        const int cu = st % 3, nx = (st + 2) % 3;
        SB();
        aq = MFMA32(fq[cu], fx[cu], aq);
        if (st + 2 < 32) { fx[nx] = ldx(st + 2); fq[nx] = ldw(st + 2, 0); }
        SB();
        ak = MFMA32(fk[cu], fx[cu], ak);
        if (st + 2 < 32) fk[nx] = ldw(st + 2, 1);
        SB();
        av = MFMA32(fx[cu], fv[cu], av);
        if (st + 2 < 32) fv[nx] = ldw(st + 2, 2);
        SB();
        if ((st & 3) == 3) STP(4 + (st >> 2));
        if ((st & 3) == 3 && st < 24) chunk_end(st >> 2);
      }
      ST(2);
      BAR();
      STP(12);
      const int T = tb * 4 + m;
      const float mean = stm[32 * m + r];
      const float rsd = str[32 * m + r];
      const float rq = rsd * (0.25f * 1.4426950408889634f);
      const float cv = csl[64 + r];
      float vq[16], vk[16], vv[16];
      f32x4 cq4[4], ck4[4], a44[4], b44[4];
#pragma unroll
      for (int g = 0; g < 4; g++) {
        cq4[g] = *(const f32x4*)(csl + 8 * g + 4 * h);
        ck4[g] = *(const f32x4*)(csl + 32 + 8 * g + 4 * h);
        a44[g] = *(const f32x4*)(str + 32 * m + 8 * g + 4 * h);
        b44[g] = *(const f32x4*)(stm + 32 * m + 8 * g + 4 * h);
      }
      SB();
#pragma unroll
      for (int g = 0; g < 4; g++) {
        const f32x4 cq = cq4[g], ck = ck4[g], a4 = a44[g], b4 = b44[g];
#pragma unroll
        for (int e = 0; e < 4; e++) {
          vk[4 * g + e] = (ak[4 * g + e] - mean * ck[e]) * rsd;
          vv[4 * g + e] = (av[4 * g + e] - b4[e] * cv) * a4[e];
          vq[4 * g + e] = (aq[4 * g + e] - mean * cq[e]) * rq;
        }
      }
      float n2[2];
#pragma unroll
      for (int hh = 0; hh < 2; hh++) {
        u32x4 pk;
        n2[hh] = 0.f;
#pragma unroll
        for (int e = 0; e < 4; e++) {
          pk[e] = pack_bf16(vk[8 * hh + 2 * e], vk[8 * hh + 2 * e + 1]);
          n2[hh] = dot2bf(pk[e], pk[e], n2[hh]);
        }
        st16(rs, WS_K + ((2 * hp + hh) * 64 + T) * 1024 + lane * 16, pk);
        *(u32x4*)(smem + L_RING + hh * 8192 + m * 1024 + lane * 16) = pk;
      }
      {
        const int hh = r >> 4, d = r & 15;
#pragma unroll
        for (int s = 0; s < 2; s++) {
          u32x4 pv;
#pragma unroll
          for (int e = 0; e < 4; e++) pv[e] = pack_bf16(vv[8 * s + 2 * e], vv[8 * s + 2 * e + 1]);
          st16(rs, WS_V + ((2 * hp + hh) * 64 + T) * 1024 + s * 512 + h * 256 + d * 16, pv);
          *(u32x4*)(smem + L_RING + hh * 8192 + 4096 + m * 1024 + s * 512 + h * 256 + d * 16) = pv;
        }
      }
#pragma unroll
      for (int hh = 0; hh < 2; hh++) {
        u32x4 pq;
#pragma unroll
        for (int e = 0; e < 4; e++) pq[e] = pack_bf16(vq[8 * hh + 2 * e], vq[8 * hh + 2 * e + 1]);
        *(u32x4*)(smem + L_Q + (hh * 4 + m) * 1024 + lane * 16) = pq;
        const float nm = max_32(add_x32(n2[hh]));
        if (lane == 0) misc[hh * 4 + m] = nm;
      }
      STP(13);
      RAW_BAR();
    } else if (wv < 8) {
      const int g = lane >> 4, rl = lane & 15;
      bf16x8 onesf;
#pragma unroll
      for (int i = 0; i < 8; i++) onesf[i] = 0x3f80;
      f32x4 gq[2], gs[2];
#pragma unroll
      for (int i = 0; i < 2; i++) { gq[i] = (f32x4){0.f, 0.f, 0.f, 0.f}; gs[i] = (f32x4){0.f, 0.f, 0.f, 0.f}; }
      unsigned xa[2][2];
#pragma unroll
      for (int i = 0; i < 2; i++)
#pragma unroll
        for (int kb = 0; kb < 2; kb++) {
          const int row = 32 * m + 16 * i + rl;
          xa[i][kb] = lbase + row * 128 + (((4 * kb + g) ^ ((row >> 1) & 7)) << 4);
        }
      auto stats = [&](int c) {
        bf16x8 f[2][2];
#pragma unroll
        for (int i = 0; i < 2; i++)
#pragma unroll
          for (int kb = 0; kb < 2; kb++) f[i][kb] = *(const LDS_AS bf16x8*)(size_t)(xa[i][kb] + (c % 5) * P1_XB);
#pragma unroll
        for (int kb = 0; kb < 2; kb++)
#pragma unroll
          for (int i = 0; i < 2; i++) { gq[i] = MFMA16(f[i][kb], f[i][kb], gq[i]); gs[i] = MFMA16(f[i][kb], onesf, gs[i]); }
      };
      stats(0);
#pragma unroll
      for (int c = 0; c < 7; c++) { stats(c + 1); if (c < 6) chunk_end(c); }
      if (g == (rl >> 2)) {
#pragma unroll
        for (int i = 0; i < 2; i++) {
          const int e = rl & 3;
          const float sq = e == 0 ? gq[i][0] : e == 1 ? gq[i][1] : e == 2 ? gq[i][2] : gq[i][3];
          const float sm = e == 0 ? gs[i][0] : e == 1 ? gs[i][1] : e == 2 ? gs[i][2] : gs[i][3];
          const float mean = sm * (1.f / D_);
          str[32 * m + 16 * i + rl] = rsqrtf(fmaxf(sq * (1.f / D_) - mean * mean, 0.f) + LN_EPS);
          stm[32 * m + 16 * i + rl] = mean;
        }
      }
      RAW_BAR();
      BAR();
    } else if (wv < 12) {
      const int g = lane >> 4;
      bf16x8 onesf;
#pragma unroll
      for (int i = 0; i < 8; i++) onesf[i] = 0x3f80;
      f32x4 cs[3];
#pragma unroll
      for (int i = 0; i < 3; i++) cs[i] = (f32x4){0.f, 0.f, 0.f, 0.f};
      unsigned wa = lbase + L_P1W + ((m & 1) * 32 + 8 * g + q) * 192 + (m >> 1) * 96 + p * 8;
      asm volatile("" : "+v"(wa));
      auto colsum = [&](int c) {
#pragma unroll
        for (int i = 0; i < 3; i++) {
          const unsigned pw = wa + (c % 5) * P1_WB + i * 32;
          const bf16x8 wf = cat8(__builtin_amdgcn_ds_read_tr16_b64_v4i16((LDS_AS s16x4*)(size_t)pw),
                                 __builtin_amdgcn_ds_read_tr16_b64_v4i16((LDS_AS s16x4*)(size_t)(pw + 4 * 192)));
          cs[i] = MFMA16(onesf, wf, cs[i]);
        }
      };
      colsum(0);
#pragma unroll
      for (int c = 0; c < 7; c++) { colsum(c + 1); if (c < 6) chunk_end(c); }
      if (lane < 16) {
#pragma unroll
        for (int i = 0; i < 3; i++) atomicAdd(csl + (m >> 1) * 48 + i * 16 + lane, cs[i][0]);
      }
      RAW_BAR();
      BAR();
    } else {
#pragma unroll
      for (int c = 0; c < 6; c++) chunk_end(c);
      BAR();
      BAR();
    }
  }
  asm volatile("s_waitcnt lgkmcnt(0)" : "+s"(tag) :: "memory");
  tag += 1u;
  ST(3);

  u32x4 xres, wrv, wr4;
  const int rb = xcd * 4 + (bi >> 3), ct = bi & 7;
  const bool is_up = bi < 28;
  const int cgp = xcd * 28 + bi;
  const int dgp = xcd * 4 + (bi - 28);
  const int tok = (tid >> 3) & 63, cg = tid & 7;
  {
    const int a = wv >> 3, kh = (wv >> 2) & 1, qt = wv & 3;
    const int head = 2 * hp + a;
    const bf16x8 qf = LD8(smem + L_Q + (a * 4 + qt) * 1024 + lane * 16);
    float q2;
    {
      u32x4 qu = __builtin_bit_cast(u32x4, qf);
      q2 = dot2bf(qu[0], qu[0], 0.f);
      q2 = dot2bf(qu[1], qu[1], q2); q2 = dot2bf(qu[2], qu[2], q2); q2 = dot2bf(qu[3], qu[3], q2);
      q2 = add_x32(q2);
    }
    const f32x4 kl = *(const f32x4*)(misc + a * 4);
    const float m0row = __builtin_amdgcn_sqrtf(q2 * fmaxf(fmaxf(kl[0], kl[1]), fmaxf(kl[2], kl[3]))) * 1.001f;
    const int kbase = L_RING + a * 8192 + kh * 2048 + lane * 16;
    const int vl = (lane >> 5) * 256 + (lane & 15) * 16;
    const bool ones_lane = (lane & 16) != 0;
    const int vbase = ones_lane ? (L_ONES + vl) : (L_RING + a * 8192 + 4096 + kh * 2048 + vl);
    const int vmask = ones_lane ? 0 : -1;
    f32x16 cinit, o;
#pragma unroll
    for (int i = 0; i < 16; i++) { cinit[i] = -m0row; o[i] = 0.f; }
    attn_seg<1>(smem + kbase, smem + vbase, vmask, qf, cinit, o);
    if (wv < 4) {
      WAIT_VM(0);
      if (lane == 0) {
        const unsigned k0 = (__float_as_uint(misc[wv]) + 0xffffu) >> 16, k1 = (__float_as_uint(misc[4 + wv]) + 0xffffu) >> 16;
        st8(rs, WS_FA + (hp * 64 + tb * 4 + wv) * 8, (u32x2){k0 | (k1 << 16), tag});
      }
    } else if (wv == 4) {
      u32x2 f;
#pragma unroll 1
      for (int it = 0; it < SPIN_MAX; it++) {
        f = ld8(rs, WS_FA + (hp * 64 + lane) * 8);
        if (__all(f[1] == tag)) break;
        SPIN_PAUSE();
      }
      const float k0 = max_64(bf_lo(f[0])), k1 = max_64(bf_hi(f[0]));
      if (lane == 0) { misc[8] = k0; misc[9] = k1; }
    }
    RAW_BAR();
    STL(4);
    const char* gsrc = ws + (((wv >> 2) & 1) ? WS_V : WS_K) + (size_t)(head * 64 + (wv & 3)) * 1024 + lane * 16;
    auto fill = [&](int s) {
      glds16_sc1(gsrc + (size_t)((tb + s) & 15) * 4096, lbase + L_RING + (s & 7) * 16384 + wv * 1024);
    };
#pragma unroll
    for (int s = 1; s < 8; s++) { fill(s); if (s == 3) BAR(); }
    {
      const float m1row = fmaxf(__builtin_amdgcn_sqrtf(q2 * misc[8 + a]) * 1.001f, m0row);
      const float f = EX2(m0row - m1row);
#pragma unroll
      for (int i = 0; i < 9; i++) o[i] *= f;
#pragma unroll
      for (int i = 0; i < 16; i++) cinit[i] = -m1row;
    }
    WAIT_VM(4);
    RAW_BAR();
    STL(5);
    attn_seg<3>(smem + kbase + 16384, smem + vbase + (vmask & 16384), vmask, qf, cinit, o);
#pragma unroll 1
    for (int u = 1; u < 4; u++) {
      if (u < 3) WAIT_VM(0); else WAIT_VM(2);
      RAW_BAR();
      if (u < 3) {
        fill(4 * u + 4); fill(4 * u + 5); fill(4 * u + 6); fill(4 * u + 7);
        if (u == 2) {
          int l2 = lane;
          asm volatile("" : "+v"(l2));
          const int slot = wv * 64 + l2;
          const unsigned short* pa = is_up ? ((slot & 1) ? wu : wg) + (size_t)(slot >> 1) * F_ + cgp * 8 : wr + l2 * 8;
          glds16(pa, lbase + L_DUMMY);
          const int pc = bi * 7 + wv, sl = pc / 56, ds = (pc - sl * 56) * 64 + l2;
          const unsigned short* pb = wv < 7 ? wd + (size_t)(ds >> 1) * D_ + (xcd * 4 + sl) * 16 + (ds & 1) * 8 : wr + l2 * 8;
          glds16(pb, lbase + L_DUMMY);
        }
      } else {
        int l2 = lane;
        asm volatile("" : "+v"(l2));
        const int rr = l2 >> 3;
        const int c = (l2 & 7) ^ (((rr >> 1) & 1) << 2);
        const unsigned short* src = wo + (size_t)(32 * wv + rr) * D_ + ct * 64 + c * 8;
#pragma unroll
        for (int i = 0; i < 4; i++) glds16(src + (size_t)i * 8 * D_, lbase + L_RING + (4 * wv + i) * 1024);
      }
      const int so = (u & 1) * 65536;
      attn_seg<4>(smem + kbase + so, smem + vbase + (vmask & so), vmask, qf, cinit, o);
    }
    __builtin_amdgcn_s_setprio(0);
    STL(6);
    float* lx = (float*)(smem + L_Q);
    float* mx = (float*)(smem + L_Q + 4096);
    float* sc = (float*)(smem + L_SC) + (a * 4 + qt) * 512;
    const int pw = wv ^ 4;
    lx[wv * 64 + lane] = o[8];
    if (kh == 1) {
#pragma unroll
      for (int i = 0; i < 8; i++) sc[i * 64 + lane] = o[i];
    }
    WAIT_VM(0);
    RAW_BAR();
    float ltot = o[8] + lx[pw * 64 + lane];
    const bool bad = !(ltot >= 9.094947e-13f);
    if (__any(bad)) {
      volatile LDS_AS int* psync = (volatile LDS_AS int*)(size_t)((unsigned)(size_t)smem + L_MISC + 512);
      auto pair_sync = [&](int phase) {
        asm volatile("s_waitcnt lgkmcnt(0)" ::: "memory");
        if (lane == 0) psync[wv] = phase;
        while (psync[pw] < phase) __builtin_amdgcn_s_sleep(1);
        MEMFENCE();
      };
      const unsigned gk = WS_K + (head * 64 + kh * 32) * 1024 + lane * 16;
      const unsigned gv = WS_V + (head * 64 + kh * 32) * 1024 + vl;
      float mt = -INFINITY;
      f32x16 zero;
#pragma unroll
      for (int i = 0; i < 16; i++) zero[i] = 0.f;
#pragma unroll 1
      for (int t = 0; t < 32; t++) {
        f32x16 sv = MFMA32(__builtin_bit_cast(bf16x8, ld16(rs, gk + t * 1024)), qf, zero);
#pragma unroll
        for (int i = 0; i < 16; i++) mt = fmaxf(mt, sv[i]);
      }
      mt = max_x32(mt);
      mx[wv * 64 + lane] = mt;
      pair_sync(1);
      mt = fmaxf(mt, mx[pw * 64 + lane]);
      const bf16x8 cst = LD8(smem + L_ONES + vl);
#pragma unroll
      for (int i = 0; i < 16; i++) { cinit[i] = -mt; o[i] = 0.f; }
#pragma unroll 1
      for (int t = 0; t < 32; t++) {
        f32x16 sv = MFMA32(__builtin_bit_cast(bf16x8, ld16(rs, gk + t * 1024)), qf, cinit);
        bf16x8 w0 = __builtin_bit_cast(bf16x8, ld16(rs, gv + t * 1024)), w1 = __builtin_bit_cast(bf16x8, ld16(rs, gv + t * 1024 + 512));
        if (ones_lane) { w0 = cst; w1 = cst; }
        bf16x8 p0, p1;
        exp_pack(sv, p0, p1);
        o = MFMA32(w0, p0, o);
        o = MFMA32(w1, p1, o);
      }
      lx[wv * 64 + lane] = o[8];
      if (kh == 1) {
#pragma unroll
        for (int i = 0; i < 8; i++) sc[i * 64 + lane] = o[i];
      }
      pair_sync(2);
      ltot = o[8] + lx[pw * 64 + lane];
    }
    if (kh == 0) {
      const float inv = 1.f / ltot;
      u32x4 po;
#pragma unroll
      for (int e = 0; e < 4; e++) {
        const float x0 = (o[2 * e] + sc[(2 * e) * 64 + lane]) * inv;
        const float x1 = (o[2 * e + 1] + sc[(2 * e + 1) * 64 + lane]) * inv;
        po[e] = pack_bf16(x0, x1);
      }
      st16(rs, WS_O + ((tb * 4 + qt) * 32 + head) * 1024 + lane * 16, po);
      WAIT_VM(0);
      if (lane == 0) st4(rs, WS_FB + ((tb * 4 + qt) * 32 + head) * 4, tag);
    }
  }
  STL(7);

  float o1[8];
  int I1 = 0, I2 = 0;
  {
    float* red = (float*)(smem + L_RED);
    if (wv < 8) {
      int t2 = tid;
      asm volatile("" : "+v"(t2));
      xres = *(const u32x4*)(x + (size_t)(rb * 64 + (t2 >> 3)) * D_ + ct * 64 + (t2 & 7) * 8);
      wrv = *(const u32x4*)(wr + ct * 64 + (t2 & 7) * 8);
      wr4 = *(const u32x4*)(wr + (t2 & 63) * 8);
    }
    if (wv < 8) {
      const int m = wv & 1, kq = wv >> 1;
      const int T = rb * 2 + m;
#pragma unroll 1
      for (int it = 0; it < SPIN_MAX; it++) {
        const unsigned f = ld4(rs, WS_FB + (T * 32 + kq * 8 + (lane & 7)) * 4);
        if (__all(f == tag)) break;
        SPIN_PAUSE();
      }
      MEMFENCE();
      STL(8);
      u32x4 of[8];
#pragma unroll
      for (int i = 0; i < 8; i++) of[i] = ld16(rs, WS_O + (T * 32 + kq * 8 + i) * 1024 + lane * 16);
      MEMFENCE();
      const int blk = (lane >> 4) & 1, q = (lane & 15) >> 2, p = lane & 3;
      f32x16 acc[2];
#pragma unroll
      for (int i = 0; i < 16; i++) { acc[0][i] = 0.f; acc[1][i] = 0.f; }
      const int rsw = ((q >> 1) & 1) << 2;
#pragma unroll
      for (int i = 0; i < 8; i++) {
        const char* rowp = smem + L_RING + (kq * 128 + i * 16 + 4 * h + q) * 128;
#pragma unroll
        for (int n = 0; n < 2; n++) {
          const int chunk = (n * 4 + blk * 2 + (p >> 1)) ^ rsw;
          const char* pw = rowp + chunk * 16 + (p & 1) * 8;
          bf16x8 wf = cat8(tr_read(pw), tr_read(pw + 8 * 128));
          acc[n] = MFMA32(wf, __builtin_bit_cast(bf16x8, of[i]), acc[n]);
        }
      }
      float* dst = red + (kq * 64 + m * 32 + r) * 68 + 4 * h;
#pragma unroll
      for (int n = 0; n < 2; n++)
#pragma unroll
        for (int g = 0; g < 4; g++)
          *(f32x4*)(dst + n * 32 + 8 * g) = (f32x4){acc[n][4 * g], acc[n][4 * g + 1], acc[n][4 * g + 2], acc[n][4 * g + 3]};
    }
    RAW_BAR();
    STL(9);
    if (wv >= 8) {
      if (is_up) {
#pragma unroll
        for (int i = 0; i < 2; i++) {
          const int slot = ((wv - 8) * 2 + i) * 64 + lane;
          const unsigned short* src = ((slot & 1) ? wu : wg) + (size_t)(slot >> 1) * F_ + cgp * 8;
          glds16(src, lbase + L_SC + ((wv - 8) * 2 + i) * 1024);
        }
      } else {
#pragma unroll
        for (int i = 0; i < 7; i++) {
          const int slot = ((wv - 8) * 7 + i) * 64 + lane;
          glds16(wd + (size_t)(slot >> 1) * D_ + dgp * 16 + (slot & 1) * 8, lbase + L_RING + ((wv - 8) * 7 + i) * 1024);
        }
      }
    }
    float rv = -INFINITY;
    u32x4 hn4;
    if (wv < 8) {
      f32x4 a0 = {0.f, 0.f, 0.f, 0.f}, a1 = {0.f, 0.f, 0.f, 0.f};
#pragma unroll
      for (int k = 0; k < 4; k++) {
        const float* src = red + (k * 64 + tok) * 68 + cg * 8;
        a0 += *(const f32x4*)src;
        a1 += *(const f32x4*)(src + 4);
      }
      u32x4 hq;
      hq[0] = pack_bf16(a0[0] + bf_lo(xres[0]), a0[1] + bf_hi(xres[0]));
      hq[1] = pack_bf16(a0[2] + bf_lo(xres[1]), a0[3] + bf_hi(xres[1]));
      hq[2] = pack_bf16(a1[0] + bf_lo(xres[2]), a1[1] + bf_hi(xres[2]));
      hq[3] = pack_bf16(a1[2] + bf_lo(xres[3]), a1[3] + bf_hi(xres[3]));
      float s1 = 0.f, s2 = 0.f, s3 = 0.f;
#pragma unroll
      for (int e = 0; e < 4; e++) {
        s1 = dot2bf(hq[e], ONES, s1);
        s2 = dot2bf(hq[e], hq[e], s2);
        s3 = dot2bf(hq[e], wrv[e], s3);
      }
      s1 = add_8(s1); s2 = add_8(s2); s3 = add_8(s3);
      if (cg == 0) st16(rs, WS_C1 + ((rb * 8 + ct) * 64 + tok) * 16, (u32x4){__float_as_uint(s1), __float_as_uint(s2), __float_as_uint(s3), tag});
      float wsum;
      {
        const u32x4 w4 = wr4;
        wsum = dot2bf(w4[0], ONES, 0.f); wsum = dot2bf(w4[1], ONES, wsum);
        wsum = dot2bf(w4[2], ONES, wsum); wsum = dot2bf(w4[3], ONES, wsum);
        wsum = add_64(wsum);
      }
      u32x4 g4;
#pragma unroll 1
      for (int it = 0; it < SPIN_MAX; it++) {
        g4 = ld16(rs, WS_C1 + ((rb * 8 + cg) * 64 + tok) * 16);
        if (__all(g4[3] == tag)) break;
        SPIN_PAUSE();
      }
      STL(10);
      const float t1 = add_8(__uint_as_float(g4[0])), t2 = add_8(__uint_as_float(g4[1])), t3 = add_8(__uint_as_float(g4[2]));
      const float mean = t1 * (1.f / D_);
      const float rstd = rsqrtf(fmaxf(t2 * (1.f / D_) - mean * mean, 0.f) + LN_EPS);
      rv = rstd * (t3 - mean * wsum);
#pragma unroll
      for (int e = 0; e < 4; e++) {
        const float h0 = bf_lo(hq[e]), h1 = bf_hi(hq[e]);
        const float n0 = (h0 - mean) * rstd, n1 = (h1 - mean) * rstd;
        hn4[e] = pack_bf16(n0, n1);
        o1[2 * e] = h0 + n0; o1[2 * e + 1] = h1 + n1;
      }
      const float M1 = max_64(rv);
      unsigned long long bm = __ballot(rv == M1);
      const int t1i = __builtin_amdgcn_readlane(tok, bm ? __builtin_ctzll(bm) : 0);
      const float c2 = (tok == t1i) ? -INFINITY : rv;
      const float M2 = max_64(c2);
      bm = __ballot(c2 == M2 && tok != t1i);
      const int t2i = __builtin_amdgcn_readlane(tok, bm ? __builtin_ctzll(bm) : 0);
      if (lane == 0) *(f32x4*)(misc + 16 + wv * 4) = (f32x4){M1, __int_as_float(t1i), M2, __int_as_float(t2i)};
    }
    RAW_BAR();
    if (wv < 8) {
      const float cv = misc[16 + ((lane & 15) >> 1) * 4 + (lane & 1) * 2];
      const int ci = __float_as_int(misc[16 + ((lane & 15) >> 1) * 4 + (lane & 1) * 2 + 1]);
      const float M1 = max_16(cv);
      unsigned long long bm = __ballot(cv == M1);
      I1 = __builtin_amdgcn_readlane(ci, bm ? __builtin_ctzll(bm) : 0);
      const float c2 = (ci == I1) ? -INFINITY : cv;
      const float M2 = max_16(c2);
      bm = __ballot(c2 == M2 && ci != I1);
      I2 = __builtin_amdgcn_readlane(ci, bm ? __builtin_ctzll(bm) : 0);
      if (I2 == I1) I2 = I1 ^ 1;
      if (tok == I1 || tok == I2) {
        const int cd = rb * 2 + (tok == I2 ? 1 : 0);
        if (ct == 0) st16(rs, WS_C2 + cg * 1024 + cd * 16, (u32x4){__float_as_uint(rv), (unsigned)(rb * 64 + tok), 0u, tag});
        const unsigned xo = WS_XS + (cd * 256 + ct * 32 + cg * 4) * 8;
        st16(rs, xo, (u32x4){hn4[0], tag, hn4[1], tag});
        st16(rs, xo + 16, (u32x4){hn4[2], tag, hn4[3], tag});
        const unsigned oo = WS_O1 + (cd * 512 + ct * 64 + cg * 8) * 8;
#pragma unroll
        for (int e = 0; e < 4; e++)
          st16(rs, oo + e * 16, (u32x4){__float_as_uint(o1[2 * e]), tag, __float_as_uint(o1[2 * e + 1]), tag});
      } else {
        u32x4 ov;
#pragma unroll
        for (int e = 0; e < 4; e++) ov[e] = pack_bf16(o1[2 * e], o1[2 * e + 1]);
        __builtin_nontemporal_store(ov, (u32x4*)(out + (size_t)(rb * 64 + tok) * D_ + ct * 64 + cg * 8));
      }
    } else if (wv == 8) {
      u32x4 g4;
#pragma unroll 1
      for (int it = 0; it < SPIN_MAX; it++) {
        g4 = ld16(rs, WS_C2 + xcd * 1024 + lane * 16);
        if (__all(g4[3] == tag)) break;
        SPIN_PAUSE();
      }
      const float cv = __uint_as_float(g4[0]);
      const float M1 = max_64(cv);
      unsigned long long bm = __ballot(cv == M1);
      const int l1 = bm ? __builtin_ctzll(bm) : 0;
      const float c2 = (lane == l1) ? -INFINITY : cv;
      const float M2 = max_64(c2);
      bm = __ballot(c2 == M2 && lane != l1);
      const int l2 = bm ? __builtin_ctzll(bm) : (l1 ^ 1);
      const int r1 = __builtin_amdgcn_readlane((int)g4[1], l1), r2 = __builtin_amdgcn_readlane((int)g4[1], l2);
      if (lane == 0) {
        *(f32x4*)(misc + 64) = (f32x4){__int_as_float(l1), __int_as_float(l2), __int_as_float(r1 & (S_ - 1)), __int_as_float(r2 & (S_ - 1))};
        misc[68] = M1; misc[69] = M2;
        if (b == 0) st4(rs, WS_EP, tag);
      }
    }
    STL(11);
    if (wv >= 8) WAIT_VM(0);
    RAW_BAR();
    STL(12);
  }
  const int cdA = __float_as_int(misc[64]), cdB = __float_as_int(misc[65]);
  const int rowA = __float_as_int(misc[66]), rowB = __float_as_int(misc[67]);
  if (wv < 8 && (tok == I1 || tok == I2)) {
    const int grow = rb * 64 + tok;
    if (grow != rowA && grow != rowB) {
      u32x4 ov;
#pragma unroll
      for (int e = 0; e < 4; e++) ov[e] = pack_bf16(o1[2 * e], o1[2 * e + 1]);
      __builtin_nontemporal_store(ov, (u32x4*)(out + (size_t)grow * D_ + ct * 64 + cg * 8));
    }
  }
  if (is_up) {
    char* xs = smem + L_RED;
    if (wv >= 8 && wv < 12) {
      const int t = tid - 512, k = t >> 7, g2 = t & 127;
      const unsigned off = WS_XS + ((k ? cdB : cdA) * 256 + g2 * 2) * 8;
      u32x4 g4;
#pragma unroll 1
      for (int it = 0; it < SPIN_MAX; it++) {
        g4 = ld16(rs, off);
        if (__all(g4[1] == tag && g4[3] == tag)) break;
        SPIN_PAUSE();
      }
      *(u32x2*)(xs + k * 1024 + g2 * 8) = (u32x2){g4[0], g4[2]};
    }
    RAW_BAR();
    STL(13);
    if (wv == 8) {
      const int g = lane >> 4, q = (lane & 15) >> 2, p = lane & 3;
      const char* xa = xs + (lane & 1) * 1024 + g * 16;
      const char* wa = smem + L_SC + (8 * g + q) * 32 + p * 8;
      f32x4 c0 = {0.f, 0.f, 0.f, 0.f}, c1 = {0.f, 0.f, 0.f, 0.f};
#pragma unroll
      for (int s = 0; s < 16; s += 2) {
        c0 = MFMA16(LD8(xa + s * 64), cat8(tr_read(wa + s * 1024), tr_read(wa + s * 1024 + 128)), c0);
        c1 = MFMA16(LD8(xa + s * 64 + 64), cat8(tr_read(wa + s * 1024 + 1024), tr_read(wa + s * 1024 + 1152)), c1);
      }
      const float g0 = c0[0] + c1[0], g1 = c0[1] + c1[1];
      const float u0 = dpp_f<0x128>(g0), u1 = dpp_f<0x128>(g1);
      if (lane < 8) {
        const unsigned gp = pack_bf16(g0, g1), up = pack_bf16(u0, u1);
        const float ga = bf_lo(gp), gb = bf_hi(gp);
        const unsigned sp = pack_bf16(ga / (1.f + __expf(-ga)), gb / (1.f + __expf(-gb)));
        const unsigned ap = pack_bf16(bf_lo(sp) * bf_lo(up), bf_hi(sp) * bf_hi(up));
        st8(rs, WS_DD + (cgp * 8 + lane) * 8, (u32x2){ap, tag});
      }
    }
  } else {
    unsigned short* act = (unsigned short*)(smem + L_RED);
    float* red2 = (float*)(smem + L_RED + 8192);
    float base = 0.f;
    if (wv == 15 && lane < 32) {
      const unsigned off = WS_O1 + (((lane >> 4) ? cdB : cdA) * 512 + dgp * 16 + (lane & 15)) * 8;
      u32x2 g2;
#pragma unroll 1
      for (int it = 0; it < SPIN_MAX; it++) {
        g2 = ld8(rs, off);
        if (__all(g2[1] == tag)) break;
        SPIN_PAUSE();
      }
      base = __uint_as_float(g2[0]);
    }
    if (wv < 14) {
      u32x4 g4;
#pragma unroll 1
      for (int it = 0; it < SPIN_MAX; it++) {
        g4 = ld16(rs, WS_DD + tid * 16);
        if (__all(g4[1] == tag && g4[3] == tag)) break;
        SPIN_PAUSE();
      }
      *(unsigned*)(act + tid * 2) = (g4[0] & 0xffffu) | (g4[2] << 16);
      *(unsigned*)(act + F_ + tid * 2) = (g4[0] >> 16) | (g4[2] & 0xffff0000u);
    }
    RAW_BAR();
    STL(14);
    if (wv < 8) {
      const int g = lane >> 4, q = (lane & 15) >> 2, p = lane & 3;
      const char* xa = (const char*)act + (lane & 1) * (F_ * 2) + g * 16 + wv * 7 * 64;
      const char* wa = smem + L_RING + (wv * 7 * 32 + 8 * g + q) * 32 + p * 8;
      f32x4 c0 = {0.f, 0.f, 0.f, 0.f};
#pragma unroll
      for (int s = 0; s < 7; s++)
        c0 = MFMA16(LD8(xa + s * 64), cat8(tr_read(wa + s * 1024), tr_read(wa + s * 1024 + 128)), c0);
      if (lane < 16) { red2[wv * 32 + lane] = c0[0]; red2[wv * 32 + 16 + lane] = c0[1]; }
    }
    RAW_BAR();
    if (wv == 15 && lane < 32) {
      float y = 0.f;
#pragma unroll
      for (int k = 0; k < 8; k++) y += red2[k * 32 + lane];
      const float rsel = misc[68 + (lane >> 4)];
      const float gate = 1.f / (1.f + __expf(-rsel));
      const int grow = (lane >> 4) ? rowB : rowA;
      out[(size_t)grow * D_ + dgp * 16 + (lane & 15)] = (unsigned short)(pack_bf16(base + y * gate, 0.f) & 0xffffu);
    }
  }
  STL(15);
  ST_FLUSH(0);
}

extern "C" void kernel_launch(void* const* d_in, const int* in_sizes, int n_in,
                              void* d_out, int out_size, void* d_ws, size_t ws_size,
                              hipStream_t stream) {
  (void)hipFuncSetAttribute((const void*)fused_block, hipFuncAttributeMaxDynamicSharedMemorySize, L_TOTAL);
  fused_block<<<256, 1024, L_TOTAL, stream>>>((const unsigned short*)d_in[0], (const unsigned short*)d_in[1],
                                             (const unsigned short*)d_in[2], (const unsigned short*)d_in[4],
                                             (const unsigned short*)d_in[5], (const unsigned short*)d_in[6],
                                             (const unsigned short*)d_in[7], (char*)d_ws, (unsigned short*)d_out);
}
```

```cpp
#include <hip/hip_runtime.h>
#include <hip/hip_bf16.h>
#include <stdint.h>


typedef __attribute__((ext_vector_type(8))) short bf16x8;
typedef __attribute__((ext_vector_type(4))) short s16x4;
typedef __attribute__((ext_vector_type(2))) __bf16 bf16x2_t;
typedef __attribute__((ext_vector_type(16))) float f32x16;
typedef __attribute__((ext_vector_type(4))) float f32x4;
typedef __attribute__((ext_vector_type(2))) float f32x2;
typedef __attribute__((ext_vector_type(4))) unsigned u32x4;
typedef __attribute__((ext_vector_type(2))) unsigned u32x2;

#define DI __device__ __forceinline__
#define LDS_AS __attribute__((address_space(3)))
#define MFMA32(a, b, c) __builtin_amdgcn_mfma_f32_32x32x16_bf16((a), (b), (c), 0, 0, 0)
#define MFMA16(a, b, c) __builtin_amdgcn_mfma_f32_16x16x32_bf16((a), (b), (c), 0, 0, 0)
#define WAIT_VM(n) asm volatile("s_waitcnt vmcnt(" #n ")" ::: "memory")
#define BAR() do { asm volatile("" ::: "memory"); __builtin_amdgcn_s_barrier(); asm volatile("" ::: "memory"); } while (0)
#define RAW_BAR() do { asm volatile("s_waitcnt lgkmcnt(0)" ::: "memory"); __builtin_amdgcn_s_barrier(); asm volatile("" ::: "memory"); } while (0)

constexpr int S_ = 2048, D_ = 512, H_ = 32, F_ = 1792;
constexpr float LN_EPS = 1e-5f;

constexpr unsigned WS_K = 0;
constexpr unsigned WS_V = WS_K + S_ * D_ * 2;
constexpr unsigned WS_O = WS_V + S_ * D_ * 2;
constexpr unsigned WS_FA = WS_O + S_ * D_ * 2;
constexpr unsigned WS_FB = WS_FA + 16 * 64 * 8;
constexpr unsigned WS_C1 = WS_FB + 64 * 32 * 4;
constexpr unsigned WS_C2 = WS_C1 + 32 * 8 * 64 * 16;
constexpr unsigned WS_XS = WS_C2 + 8 * 32 * 2 * 16;
constexpr unsigned WS_O1 = WS_XS + 32 * 2 * 256 * 8;
constexpr unsigned WS_DD = WS_O1 + 32 * 2 * 512 * 8;
constexpr unsigned WS_EP = WS_DD + F_ * 8;
constexpr unsigned WS_END = WS_EP + 256;

DI unsigned pack_bf16(float a, float b) {
  f32x2 v = {a, b};
  bf16x2_t r = __builtin_convertvector(v, bf16x2_t);
  return __builtin_bit_cast(unsigned, r);
}
DI float bf_lo(unsigned u) { return __uint_as_float(u << 16); }
DI float bf_hi(unsigned u) { return __uint_as_float(u & 0xffff0000u); }
DI float dot2bf(unsigned a, unsigned b, float c) {
  return __builtin_amdgcn_fdot2_f32_bf16(__builtin_bit_cast(bf16x2_t, a), __builtin_bit_cast(bf16x2_t, b), c, false);
}
DI void glds16(const void* g, unsigned lds_base) {
  unsigned keep;
  asm volatile("s_mov_b32 %0, m0\n\ts_mov_b32 m0, %2\n\ts_nop 0\n\tglobal_load_lds_dwordx4 %1, off\n\ts_mov_b32 m0, %0"
               : "=&s"(keep) : "v"(g), "s"(lds_base) : "memory");
}
DI void glds16_sc1(const void* g, unsigned lds_base) {
  unsigned keep;
  asm volatile("s_mov_b32 %0, m0\n\ts_mov_b32 m0, %2\n\ts_nop 0\n\tglobal_load_lds_dwordx4 %1, off sc1\n\ts_mov_b32 m0, %0"
               : "=&s"(keep) : "v"(g), "s"(lds_base) : "memory");
}
DI s16x4 tr_read(const char* p) {
  return __builtin_amdgcn_ds_read_tr16_b64_v4i16((LDS_AS s16x4*)(size_t)(unsigned)(size_t)p);
}
DI bf16x8 cat8(s16x4 lo, s16x4 hi) { return __builtin_shufflevector(lo, hi, 0, 1, 2, 3, 4, 5, 6, 7); }
template <int CTRL> DI float dpp_f(float x) {
  return __builtin_bit_cast(float, __builtin_amdgcn_update_dpp(0, __builtin_bit_cast(int, x), CTRL, 0xF, 0xF, true));
}
DI float add_x32(float v) {
  auto r = __builtin_amdgcn_permlane32_swap(__float_as_uint(v), __float_as_uint(v), false, false);
  return __uint_as_float(r[0]) + __uint_as_float(r[1]);
}
DI float max_x32(float v) {
  auto r = __builtin_amdgcn_permlane32_swap(__float_as_uint(v), __float_as_uint(v), false, false);
  return fmaxf(__uint_as_float(r[0]), __uint_as_float(r[1]));
}
DI float add_x16(float v) {
  auto r = __builtin_amdgcn_permlane16_swap(__float_as_uint(v), __float_as_uint(v), false, false);
  return __uint_as_float(r[0]) + __uint_as_float(r[1]);
}
DI float max_x16(float v) {
  auto r = __builtin_amdgcn_permlane16_swap(__float_as_uint(v), __float_as_uint(v), false, false);
  return fmaxf(__uint_as_float(r[0]), __uint_as_float(r[1]));
}
DI float add_8(float v) { v += dpp_f<0xB1>(v); v += dpp_f<0x4E>(v); v += dpp_f<0x141>(v); return v; }
DI float add_16(float v) { v = add_8(v); v += dpp_f<0x140>(v); return v; }
DI float add_64(float v) { return add_x32(add_x16(add_16(v))); }
DI float max_16(float v) {
  v = fmaxf(v, dpp_f<0xB1>(v)); v = fmaxf(v, dpp_f<0x4E>(v)); v = fmaxf(v, dpp_f<0x141>(v)); v = fmaxf(v, dpp_f<0x140>(v));
  return v;
}
DI float max_32(float v) { return max_x16(max_16(v)); }
DI float max_64(float v) { return max_x32(max_32(v)); }

DI void exp_pack(const f32x16& s, bf16x8& p0, bf16x8& p1) {
  u32x4 a, c;
#pragma unroll
  for (int e = 0; e < 4; e++) {
    a[e] = pack_bf16(__builtin_amdgcn_exp2f(s[2 * e]), __builtin_amdgcn_exp2f(s[2 * e + 1]));
    c[e] = pack_bf16(__builtin_amdgcn_exp2f(s[8 + 2 * e]), __builtin_amdgcn_exp2f(s[8 + 2 * e + 1]));
  }
  p0 = __builtin_bit_cast(bf16x8, a);
  p1 = __builtin_bit_cast(bf16x8, c);
}
#define LD8(p) (*(const bf16x8*)(p))
#define SB() __builtin_amdgcn_sched_barrier(0)
#define EX2(x) __builtin_amdgcn_exp2f(x)

template <int NS>
DI void attn_seg(const char* ka, const char* va, int vmask, const bf16x8 qf, const f32x16& cinit, f32x16& o) {
  constexpr int NT = 2 * NS;
  bf16x8 kn = LD8(ka + 1024);
  f32x16 s = MFMA32(LD8(ka), qf, cinit);
  bf16x8 v0 = LD8(va), v1 = LD8(va + 512);
  bf16x8 vprev, pprev;
#pragma unroll
  for (int j = 0; j < NT; j++) {
    if ((j & 1) == 0) {
      if (NS - 1 - (j >> 1) == 3) __builtin_amdgcn_s_setprio(3);
      else if (NS - 1 - (j >> 1) == 2) __builtin_amdgcn_s_setprio(2);
      else if (NS - 1 - (j >> 1) == 1) __builtin_amdgcn_s_setprio(1);
      else __builtin_amdgcn_s_setprio(0);
    }
    const char* vp = va + (vmask & (((j + 1) >> 1) * 16384)) + ((j + 1) & 1) * 1024;
    const char* kp = ka + ((j + 2) >> 1) * 16384 + ((j + 2) & 1) * 1024;
    float e0[8], e1[8];
    u32x4 pa, pc;
    f32x16 sNext;
    bf16x8 w0, w1, k2;
    SB();
#pragma unroll
    for (int e = 0; e < 4; e++) e0[e] = EX2(s[e]);
    SB();
    if (j > 0) o = MFMA32(vprev, pprev, o);
    SB();
#pragma unroll
    for (int e = 4; e < 8; e++) e0[e] = EX2(s[e]);
#pragma unroll
    for (int e = 0; e < 4; e++) pa[e] = pack_bf16(e0[2 * e], e0[2 * e + 1]);
    SB();
    if (j + 1 < NT) { sNext = MFMA32(kn, qf, cinit); w0 = LD8(vp); w1 = LD8(vp + 512); }
    if (j + 2 < NT) k2 = LD8(kp);
    SB();
#pragma unroll
    for (int e = 0; e < 5; e++) e1[e] = EX2(s[8 + e]);
    SB();
    o = MFMA32(v0, __builtin_bit_cast(bf16x8, pa), o);
    SB();
#pragma unroll
    for (int e = 5; e < 8; e++) e1[e] = EX2(s[8 + e]);
#pragma unroll
    for (int e = 0; e < 4; e++) pc[e] = pack_bf16(e1[2 * e], e1[2 * e + 1]);
    pprev = __builtin_bit_cast(bf16x8, pc); vprev = v1;
    if (j + 1 < NT) { s = sNext; v0 = w0; v1 = w1; }
    if (j + 2 < NT) kn = k2;
  }
  SB();
  o = MFMA32(vprev, pprev, o);
}


typedef __amdgpu_buffer_rsrc_t rsrc_t;
DI void st16(rsrc_t r, unsigned off, u32x4 v) { __builtin_amdgcn_raw_buffer_store_b128(v, r, off, 0, 16); }
DI void st8(rsrc_t r, unsigned off, u32x2 v) { __builtin_amdgcn_raw_buffer_store_b64(v, r, off, 0, 16); }
DI void st4(rsrc_t r, unsigned off, unsigned v) { __builtin_amdgcn_raw_buffer_store_b32(v, r, off, 0, 16); }
DI u32x4 ld16(rsrc_t r, unsigned off) { asm volatile("" : "+v"(off) :: "memory"); return __builtin_amdgcn_raw_buffer_load_b128(r, off, 0, 16); }
DI u32x2 ld8(rsrc_t r, unsigned off) { asm volatile("" : "+v"(off) :: "memory"); return __builtin_amdgcn_raw_buffer_load_b64(r, off, 0, 16); }
DI unsigned ld4(rsrc_t r, unsigned off) { asm volatile("" : "+v"(off) :: "memory"); return __builtin_amdgcn_raw_buffer_load_b32(r, off, 0, 16); }
#define MEMFENCE() asm volatile("" ::: "memory")
constexpr int SPIN_MAX = 100000;
#define SPIN_PAUSE() do { __builtin_amdgcn_s_sleep(2); MEMFENCE(); } while (0)

constexpr int L_RING = 0;
constexpr int P1_XB = 128 * 64 * 2, P1_WB = 64 * 96 * 2;
constexpr int L_P1W = 5 * P1_XB;
constexpr int L_RED = 65536;
constexpr int L_DUMMY = 131072;
constexpr int L_SC = 135168;
constexpr int L_ONES = 151552;
constexpr int L_Q = L_ONES + 2048;
constexpr int L_MISC = L_Q + 8192;
constexpr int L_P1S = L_MISC + 1024;
constexpr int L_TOTAL = L_P1S + 1024;

__global__ __launch_bounds__(1024) void fused_block(const unsigned short* __restrict__ x, const unsigned short* __restrict__ wqkv,
                                                    const unsigned short* __restrict__ wo, const unsigned short* __restrict__ wr,
                                                    const unsigned short* __restrict__ wg, const unsigned short* __restrict__ wu,
                                                    const unsigned short* __restrict__ wd, char* ws,
                                                    unsigned short* __restrict__ out) {
  extern __shared__ __attribute__((aligned(16))) char smem[];
  const int tid = threadIdx.x, lane = tid & 63;
  const int wv = __builtin_amdgcn_readfirstlane(tid >> 6);
  const int b = blockIdx.x, xcd = b & 7, bi = b >> 3;
  const int hp = 2 * xcd + (bi & 1), tb = bi >> 1;
  const rsrc_t rs = __builtin_amdgcn_make_buffer_rsrc(ws, 0, WS_END, 0x00020000);
  const int r = lane & 31, h = lane >> 5;
  const unsigned ONES = 0x3f803f80u;
  float* misc = (float*)(smem + L_MISC);
  const unsigned lbase = __builtin_amdgcn_readfirstlane((unsigned)(size_t)smem);
  unsigned tag;

  {
    const int m0 = tb * 128;
    const int m = wv & 3;
    float* csl = misc + 160;
    float* str = (float*)(smem + L_P1S);
    float* stm = str + 128;
    const unsigned short* xsrc;
    const unsigned short* wsrc;
    {
      const int row = 8 * wv + (lane >> 3);
      xsrc = x + (size_t)(m0 + row) * D_ + ((lane & 7) ^ ((row >> 1) & 7)) * 8;
      const int l48 = lane < 48 ? lane : 0;
      const int kr = l48 / 12, sq = l48 - kr * 12;
      wsrc = wqkv + (size_t)(4 * wv + kr) * (3 * D_) + (sq >> 2) * D_ + hp * 32 + (sq & 3) * 8;
    }
    auto stage = [&](int c) {
      glds16(xsrc + c * 64, lbase + (c % 5) * P1_XB + wv * 1024);
      if (lane < 48) glds16(wsrc + (size_t)c * 64 * (3 * D_), lbase + L_P1W + (c % 5) * P1_WB + wv * 768);
    };
    auto chunk_end = [&](int c) {
      if (c <= 3) WAIT_VM(4); else if (c == 4) WAIT_VM(2); else WAIT_VM(0);
      BAR();
      if (c + 5 < 8) stage(c + 5);
    };
    stage(0); BAR(); stage(1); BAR(); stage(2); stage(3); stage(4);
    asm volatile("s_load_dword %0, %1, 0x0\n\ts_waitcnt lgkmcnt(0)" : "=s"(tag) : "s"(ws + WS_EP) : "memory");
    tag += 1u;
    if (wv == 15) {
      const unsigned o1v = ((lane & 15) == 0 || (lane & 15) == 4) ? ONES : 0u;
      *(u32x4*)(smem + L_ONES + lane * 16) = (u32x4){o1v, o1v, o1v, o1v};
      *(u32x4*)(smem + L_ONES + 1024 + lane * 16) = (u32x4){o1v, o1v, o1v, o1v};
      ((int*)misc)[128 + lane] = 0; ((int*)misc)[192 + lane] = 0;
    }
    const int q = (lane & 15) >> 2, p = lane & 3;
    WAIT_VM(6);
    RAW_BAR();
    if (wv < 4) {
      const int xsw = (r >> 1) & 7;
      const int blk = (lane >> 4) & 1;
      unsigned wb = lbase + L_P1W + (8 * h + q) * 192 + blk * 32 + p * 8;
      asm volatile("" : "+v"(wb));
      unsigned xb[2][4];
#pragma unroll
      for (int ks = 0; ks < 4; ks++) {
        xb[0][ks] = lbase + (32 * m + r) * 128 + (((2 * ks + h) ^ xsw) << 4);
        xb[1][ks] = xb[0][ks] + 65536;
        asm volatile("" : "+v"(xb[0][ks]), "+v"(xb[1][ks]));
      }
      auto ldx = [&](int st) {
        const int off = ((st >> 2) % 5) * P1_XB;
        return *(const LDS_AS bf16x8*)(size_t)(xb[off >= 65536][st & 3] + (off & 65535));
      };
      auto ldw = [&](int st, int sg) {
        const unsigned pw = wb + ((st >> 2) % 5) * P1_WB + (st & 3) * 16 * 192 + sg * 64;
        return cat8(__builtin_amdgcn_ds_read_tr16_b64_v4i16((LDS_AS s16x4*)(size_t)pw),
                    __builtin_amdgcn_ds_read_tr16_b64_v4i16((LDS_AS s16x4*)(size_t)(pw + 4 * 192)));
      };
      f32x16 aq, ak, av;
#pragma unroll
      for (int i = 0; i < 16; i++) { aq[i] = 0.f; ak[i] = 0.f; av[i] = 0.f; }
      bf16x8 fx[3], fq[3], fk[3], fv[3];
#pragma unroll
      for (int st = 0; st < 2; st++) { fx[st] = ldx(st); fq[st] = ldw(st, 0); fk[st] = ldw(st, 1); fv[st] = ldw(st, 2); }
#pragma unroll
      for (int st = 0; st < 32; st++) {
        const int cu = st % 3, nx = (st + 2) % 3;
        SB();
        aq = MFMA32(fq[cu], fx[cu], aq);
        if (st + 2 < 32) { fx[nx] = ldx(st + 2); fq[nx] = ldw(st + 2, 0); }
        SB();
        ak = MFMA32(fk[cu], fx[cu], ak);
        if (st + 2 < 32) fk[nx] = ldw(st + 2, 1);
        SB();
        av = MFMA32(fx[cu], fv[cu], av);
        if (st + 2 < 32) fv[nx] = ldw(st + 2, 2);
        SB();
        if ((st & 3) == 3 && st < 24) chunk_end(st >> 2);
      }
      BAR();
      const int T = tb * 4 + m;
      const float mean = stm[32 * m + r];
      const float rsd = str[32 * m + r];
      const float rq = rsd * (0.25f * 1.4426950408889634f);
      const float cv = csl[64 + r];
      float vq[16], vk[16], vv[16];
      f32x4 cq4[4], ck4[4], a44[4], b44[4];
#pragma unroll
      for (int g = 0; g < 4; g++) {
        cq4[g] = *(const f32x4*)(csl + 8 * g + 4 * h);
        ck4[g] = *(const f32x4*)(csl + 32 + 8 * g + 4 * h);
        a44[g] = *(const f32x4*)(str + 32 * m + 8 * g + 4 * h);
        b44[g] = *(const f32x4*)(stm + 32 * m + 8 * g + 4 * h);
      }
      SB();
#pragma unroll
      for (int g = 0; g < 4; g++) {
        const f32x4 cq = cq4[g], ck = ck4[g], a4 = a44[g], b4 = b44[g];
#pragma unroll
        for (int e = 0; e < 4; e++) {
          vk[4 * g + e] = (ak[4 * g + e] - mean * ck[e]) * rsd;
          vv[4 * g + e] = (av[4 * g + e] - b4[e] * cv) * a4[e];
          vq[4 * g + e] = (aq[4 * g + e] - mean * cq[e]) * rq;
        }
      }
      float n2[2];
#pragma unroll
      for (int hh = 0; hh < 2; hh++) {
        u32x4 pk;
        n2[hh] = 0.f;
#pragma unroll
        for (int e = 0; e < 4; e++) {
          pk[e] = pack_bf16(vk[8 * hh + 2 * e], vk[8 * hh + 2 * e + 1]);
          n2[hh] = dot2bf(pk[e], pk[e], n2[hh]);
        }
        st16(rs, WS_K + ((2 * hp + hh) * 64 + T) * 1024 + lane * 16, pk);
        *(u32x4*)(smem + L_RING + hh * 8192 + m * 1024 + lane * 16) = pk;
      }
      {
        const int hh = r >> 4, d = r & 15;
#pragma unroll
        for (int s = 0; s < 2; s++) {
          u32x4 pv;
#pragma unroll
          for (int e = 0; e < 4; e++) pv[e] = pack_bf16(vv[8 * s + 2 * e], vv[8 * s + 2 * e + 1]);
          st16(rs, WS_V + ((2 * hp + hh) * 64 + T) * 1024 + s * 512 + h * 256 + d * 16, pv);
          *(u32x4*)(smem + L_RING + hh * 8192 + 4096 + m * 1024 + s * 512 + h * 256 + d * 16) = pv;
        }
      }
#pragma unroll
      for (int hh = 0; hh < 2; hh++) {
        u32x4 pq;
#pragma unroll
        for (int e = 0; e < 4; e++) pq[e] = pack_bf16(vq[8 * hh + 2 * e], vq[8 * hh + 2 * e + 1]);
        *(u32x4*)(smem + L_Q + (hh * 4 + m) * 1024 + lane * 16) = pq;
        const float nm = max_32(add_x32(n2[hh]));
        if (lane == 0) misc[hh * 4 + m] = nm;
      }
      RAW_BAR();
    } else if (wv < 8) {
      const int g = lane >> 4, rl = lane & 15;
      bf16x8 onesf;
#pragma unroll
      for (int i = 0; i < 8; i++) onesf[i] = 0x3f80;
      f32x4 gq[2], gs[2];
#pragma unroll
      for (int i = 0; i < 2; i++) { gq[i] = (f32x4){0.f, 0.f, 0.f, 0.f}; gs[i] = (f32x4){0.f, 0.f, 0.f, 0.f}; }
      unsigned xa[2][2];
#pragma unroll
      for (int i = 0; i < 2; i++)
#pragma unroll
        for (int kb = 0; kb < 2; kb++) {
          const int row = 32 * m + 16 * i + rl;
          xa[i][kb] = lbase + row * 128 + (((4 * kb + g) ^ ((row >> 1) & 7)) << 4);
        }
      auto stats = [&](int c) {
        bf16x8 f[2][2];
#pragma unroll
        for (int i = 0; i < 2; i++)
#pragma unroll
          for (int kb = 0; kb < 2; kb++) f[i][kb] = *(const LDS_AS bf16x8*)(size_t)(xa[i][kb] + (c % 5) * P1_XB);
#pragma unroll
        for (int kb = 0; kb < 2; kb++)
#pragma unroll
          for (int i = 0; i < 2; i++) { gq[i] = MFMA16(f[i][kb], f[i][kb], gq[i]); gs[i] = MFMA16(f[i][kb], onesf, gs[i]); }
      };
      stats(0);
#pragma unroll
      for (int c = 0; c < 7; c++) { stats(c + 1); if (c < 6) chunk_end(c); }
      if (g == (rl >> 2)) {
#pragma unroll
        for (int i = 0; i < 2; i++) {
          const int e = rl & 3;
          const float sq = e == 0 ? gq[i][0] : e == 1 ? gq[i][1] : e == 2 ? gq[i][2] : gq[i][3];
          const float sm = e == 0 ? gs[i][0] : e == 1 ? gs[i][1] : e == 2 ? gs[i][2] : gs[i][3];
          const float mean = sm * (1.f / D_);
          str[32 * m + 16 * i + rl] = rsqrtf(fmaxf(sq * (1.f / D_) - mean * mean, 0.f) + LN_EPS);
          stm[32 * m + 16 * i + rl] = mean;
        }
      }
      RAW_BAR();
      BAR();
    } else if (wv < 12) {
      const int g = lane >> 4;
      bf16x8 onesf;
#pragma unroll
      for (int i = 0; i < 8; i++) onesf[i] = 0x3f80;
      f32x4 cs[3];
#pragma unroll
      for (int i = 0; i < 3; i++) cs[i] = (f32x4){0.f, 0.f, 0.f, 0.f};
      unsigned wa = lbase + L_P1W + ((m & 1) * 32 + 8 * g + q) * 192 + (m >> 1) * 96 + p * 8;
      asm volatile("" : "+v"(wa));
      auto colsum = [&](int c) {
#pragma unroll
        for (int i = 0; i < 3; i++) {
          const unsigned pw = wa + (c % 5) * P1_WB + i * 32;
          const bf16x8 wf = cat8(__builtin_amdgcn_ds_read_tr16_b64_v4i16((LDS_AS s16x4*)(size_t)pw),
                                 __builtin_amdgcn_ds_read_tr16_b64_v4i16((LDS_AS s16x4*)(size_t)(pw + 4 * 192)));
          cs[i] = MFMA16(onesf, wf, cs[i]);
        }
      };
      colsum(0);
#pragma unroll
      for (int c = 0; c < 7; c++) { colsum(c + 1); if (c < 6) chunk_end(c); }
      if (lane < 16) {
#pragma unroll
        for (int i = 0; i < 3; i++) atomicAdd(csl + (m >> 1) * 48 + i * 16 + lane, cs[i][0]);
      }
      RAW_BAR();
      BAR();
    } else {
#pragma unroll
      for (int c = 0; c < 6; c++) chunk_end(c);
      BAR();
      BAR();
    }
  }

  u32x4 xres, wrv, wr4;
  const int rb = xcd * 4 + (bi >> 3), ct = bi & 7;
  const bool is_up = bi < 28;
  const int cgp = xcd * 28 + bi;
  const int dgp = xcd * 4 + (bi - 28);
  const int tok = (tid >> 3) & 63, cg = tid & 7;
  {
    const int a = wv >> 3, kh = (wv >> 2) & 1, qt = wv & 3;
    const int head = 2 * hp + a;
    const bf16x8 qf = LD8(smem + L_Q + (a * 4 + qt) * 1024 + lane * 16);
    float q2;
    {
      u32x4 qu = __builtin_bit_cast(u32x4, qf);
      q2 = dot2bf(qu[0], qu[0], 0.f);
      q2 = dot2bf(qu[1], qu[1], q2); q2 = dot2bf(qu[2], qu[2], q2); q2 = dot2bf(qu[3], qu[3], q2);
      q2 = add_x32(q2);
    }
    const f32x4 kl = *(const f32x4*)(misc + a * 4);
    const float m0row = __builtin_amdgcn_sqrtf(q2 * fmaxf(fmaxf(kl[0], kl[1]), fmaxf(kl[2], kl[3]))) * 1.001f;
    const int kbase = L_RING + a * 8192 + kh * 2048 + lane * 16;
    const int vl = (lane >> 5) * 256 + (lane & 15) * 16;
    const bool ones_lane = (lane & 16) != 0;
    const int vbase = ones_lane ? (L_ONES + vl) : (L_RING + a * 8192 + 4096 + kh * 2048 + vl);
    const int vmask = ones_lane ? 0 : -1;
    f32x16 cinit, o;
#pragma unroll
    for (int i = 0; i < 16; i++) { cinit[i] = -m0row; o[i] = 0.f; }
    attn_seg<1>(smem + kbase, smem + vbase, vmask, qf, cinit, o);
    if (wv < 4) {
      WAIT_VM(0);
      if (lane == 0) {
        const unsigned k0 = (__float_as_uint(misc[wv]) + 0xffffu) >> 16, k1 = (__float_as_uint(misc[4 + wv]) + 0xffffu) >> 16;
        st8(rs, WS_FA + (hp * 64 + tb * 4 + wv) * 8, (u32x2){k0 | (k1 << 16), tag});
      }
    } else if (wv == 4) {
      u32x2 f;
#pragma unroll 1
      for (int it = 0; it < SPIN_MAX; it++) {
        f = ld8(rs, WS_FA + (hp * 64 + lane) * 8);
        if (__all(f[1] == tag)) break;
        SPIN_PAUSE();
      }
      const float k0 = max_64(bf_lo(f[0])), k1 = max_64(bf_hi(f[0]));
      if (lane == 0) { misc[8] = k0; misc[9] = k1; }
    }
    RAW_BAR();
    const char* gsrc = ws + (((wv >> 2) & 1) ? WS_V : WS_K) + (size_t)(head * 64 + (wv & 3)) * 1024 + lane * 16;
    auto fill = [&](int s) {
      glds16_sc1(gsrc + (size_t)((tb + s) & 15) * 4096, lbase + L_RING + (s & 7) * 16384 + wv * 1024);
    };
#pragma unroll
    for (int s = 1; s < 8; s++) { fill(s); if (s == 3) BAR(); }
    {
      const float m1row = fmaxf(__builtin_amdgcn_sqrtf(q2 * misc[8 + a]) * 1.001f, m0row);
      const float f = EX2(m0row - m1row);
#pragma unroll
      for (int i = 0; i < 9; i++) o[i] *= f;
#pragma unroll
      for (int i = 0; i < 16; i++) cinit[i] = -m1row;
    }
    WAIT_VM(4);
    RAW_BAR();
    attn_seg<3>(smem + kbase + 16384, smem + vbase + (vmask & 16384), vmask, qf, cinit, o);
#pragma unroll 1
    for (int u = 1; u < 4; u++) {
      if (u < 3) WAIT_VM(0); else WAIT_VM(2);
      RAW_BAR();
      if (u < 3) {
        fill(4 * u + 4); fill(4 * u + 5); fill(4 * u + 6); fill(4 * u + 7);
        if (u == 2) {
          int l2 = lane;
          asm volatile("" : "+v"(l2));
          const int slot = wv * 64 + l2;
          const unsigned short* pa = is_up ? ((slot & 1) ? wu : wg) + (size_t)(slot >> 1) * F_ + cgp * 8 : wr + l2 * 8;
          glds16(pa, lbase + L_DUMMY);
          const int pc = bi * 7 + wv, sl = pc / 56, ds = (pc - sl * 56) * 64 + l2;
          const unsigned short* pb = wv < 7 ? wd + (size_t)(ds >> 1) * D_ + (xcd * 4 + sl) * 16 + (ds & 1) * 8 : wr + l2 * 8;
          glds16(pb, lbase + L_DUMMY);
        }
      } else {
        int l2 = lane;
        asm volatile("" : "+v"(l2));
        const int rr = l2 >> 3;
        const int c = (l2 & 7) ^ (((rr >> 1) & 1) << 2);
        const unsigned short* src = wo + (size_t)(32 * wv + rr) * D_ + ct * 64 + c * 8;
#pragma unroll
        for (int i = 0; i < 4; i++) glds16(src + (size_t)i * 8 * D_, lbase + L_RING + (4 * wv + i) * 1024);
      }
      const int so = (u & 1) * 65536;
      attn_seg<4>(smem + kbase + so, smem + vbase + (vmask & so), vmask, qf, cinit, o);
    }
    __builtin_amdgcn_s_setprio(0);
    float* lx = (float*)(smem + L_Q);
    float* mx = (float*)(smem + L_Q + 4096);
    float* sc = (float*)(smem + L_SC) + (a * 4 + qt) * 512;
    const int pw = wv ^ 4;
    lx[wv * 64 + lane] = o[8];
    if (kh == 1) {
#pragma unroll
      for (int i = 0; i < 8; i++) sc[i * 64 + lane] = o[i];
    }
    WAIT_VM(0);
    RAW_BAR();
    float ltot = o[8] + lx[pw * 64 + lane];
    const bool bad = !(ltot >= 8.6736174e-19f);
    if (__any(bad)) {
      volatile LDS_AS int* psync = (volatile LDS_AS int*)(size_t)((unsigned)(size_t)smem + L_MISC + 512);
      auto pair_sync = [&](int phase) {
        asm volatile("s_waitcnt lgkmcnt(0)" ::: "memory");
        if (lane == 0) psync[wv] = phase;
        while (psync[pw] < phase) __builtin_amdgcn_s_sleep(1);
        MEMFENCE();
      };
      const unsigned gk = WS_K + (head * 64 + kh * 32) * 1024 + lane * 16;
      const unsigned gv = WS_V + (head * 64 + kh * 32) * 1024 + vl;
      float mt = -INFINITY;
      f32x16 zero;
#pragma unroll
      for (int i = 0; i < 16; i++) zero[i] = 0.f;
#pragma unroll 1
      for (int t = 0; t < 32; t++) {
        f32x16 sv = MFMA32(__builtin_bit_cast(bf16x8, ld16(rs, gk + t * 1024)), qf, zero);
#pragma unroll
        for (int i = 0; i < 16; i++) mt = fmaxf(mt, sv[i]);
      }
      mt = max_x32(mt);
      mx[wv * 64 + lane] = mt;
      pair_sync(1);
      mt = fmaxf(mt, mx[pw * 64 + lane]);
      const bf16x8 cst = LD8(smem + L_ONES + vl);
#pragma unroll
      for (int i = 0; i < 16; i++) { cinit[i] = -mt; o[i] = 0.f; }
#pragma unroll 1
      for (int t = 0; t < 32; t++) {
        f32x16 sv = MFMA32(__builtin_bit_cast(bf16x8, ld16(rs, gk + t * 1024)), qf, cinit);
        bf16x8 w0 = __builtin_bit_cast(bf16x8, ld16(rs, gv + t * 1024)), w1 = __builtin_bit_cast(bf16x8, ld16(rs, gv + t * 1024 + 512));
        if (ones_lane) { w0 = cst; w1 = cst; }
        bf16x8 p0, p1;
        exp_pack(sv, p0, p1);
        o = MFMA32(w0, p0, o);
        o = MFMA32(w1, p1, o);
      }
      lx[wv * 64 + lane] = o[8];
      if (kh == 1) {
#pragma unroll
        for (int i = 0; i < 8; i++) sc[i * 64 + lane] = o[i];
      }
      pair_sync(2);
      ltot = o[8] + lx[pw * 64 + lane];
    }
    if (kh == 0) {
      const float inv = 1.f / ltot;
      u32x4 po;
#pragma unroll
      for (int e = 0; e < 4; e++) {
        const float x0 = (o[2 * e] + sc[(2 * e) * 64 + lane]) * inv;
        const float x1 = (o[2 * e + 1] + sc[(2 * e + 1) * 64 + lane]) * inv;
        po[e] = pack_bf16(x0, x1);
      }
      st16(rs, WS_O + ((tb * 4 + qt) * 32 + head) * 1024 + lane * 16, po);
      WAIT_VM(0);
      if (lane == 0) st4(rs, WS_FB + ((tb * 4 + qt) * 32 + head) * 4, tag);
    }
  }

  float o1[8];
  int I1 = 0, I2 = 0;
  {
    float* red = (float*)(smem + L_RED);
    if (wv < 8) {
      int t2 = tid;
      asm volatile("" : "+v"(t2));
      xres = *(const u32x4*)(x + (size_t)(rb * 64 + (t2 >> 3)) * D_ + ct * 64 + (t2 & 7) * 8);
      wrv = *(const u32x4*)(wr + ct * 64 + (t2 & 7) * 8);
      wr4 = *(const u32x4*)(wr + (t2 & 63) * 8);
    }
    if (wv < 8) {
      const int m = wv & 1, kq = wv >> 1;
      const int T = rb * 2 + m;
#pragma unroll 1
      for (int it = 0; it < SPIN_MAX; it++) {
        const unsigned f = ld4(rs, WS_FB + (T * 32 + kq * 8 + (lane & 7)) * 4);
        if (__all(f == tag)) break;
        SPIN_PAUSE();
      }
      MEMFENCE();
      u32x4 of[8];
#pragma unroll
      for (int i = 0; i < 8; i++) of[i] = ld16(rs, WS_O + (T * 32 + kq * 8 + i) * 1024 + lane * 16);
      MEMFENCE();
      const int blk = (lane >> 4) & 1, q = (lane & 15) >> 2, p = lane & 3;
      f32x16 acc[2];
#pragma unroll
      for (int i = 0; i < 16; i++) { acc[0][i] = 0.f; acc[1][i] = 0.f; }
      const int rsw = ((q >> 1) & 1) << 2;
#pragma unroll
      for (int i = 0; i < 8; i++) {
        const char* rowp = smem + L_RING + (kq * 128 + i * 16 + 4 * h + q) * 128;
#pragma unroll
        for (int n = 0; n < 2; n++) {
          const int chunk = (n * 4 + blk * 2 + (p >> 1)) ^ rsw;
          const char* pw = rowp + chunk * 16 + (p & 1) * 8;
          bf16x8 wf = cat8(tr_read(pw), tr_read(pw + 8 * 128));
          acc[n] = MFMA32(wf, __builtin_bit_cast(bf16x8, of[i]), acc[n]);
        }
      }
      float* dst = red + (kq * 64 + m * 32 + r) * 68 + 4 * h;
#pragma unroll
      for (int n = 0; n < 2; n++)
#pragma unroll
        for (int g = 0; g < 4; g++)
          *(f32x4*)(dst + n * 32 + 8 * g) = (f32x4){acc[n][4 * g], acc[n][4 * g + 1], acc[n][4 * g + 2], acc[n][4 * g + 3]};
    }
    RAW_BAR();
    if (wv >= 8) {
      if (is_up) {
#pragma unroll
        for (int i = 0; i < 2; i++) {
          const int slot = ((wv - 8) * 2 + i) * 64 + lane;
          const unsigned short* src = ((slot & 1) ? wu : wg) + (size_t)(slot >> 1) * F_ + cgp * 8;
          glds16(src, lbase + L_SC + ((wv - 8) * 2 + i) * 1024);
        }
      } else {
#pragma unroll
        for (int i = 0; i < 7; i++) {
          const int slot = ((wv - 8) * 7 + i) * 64 + lane;
          glds16(wd + (size_t)(slot >> 1) * D_ + dgp * 16 + (slot & 1) * 8, lbase + L_RING + ((wv - 8) * 7 + i) * 1024);
        }
      }
    }
    float rv = -INFINITY;
    u32x4 hn4;
    if (wv < 8) {
      f32x4 a0 = {0.f, 0.f, 0.f, 0.f}, a1 = {0.f, 0.f, 0.f, 0.f};
#pragma unroll
      for (int k = 0; k < 4; k++) {
        const float* src = red + (k * 64 + tok) * 68 + cg * 8;
        a0 += *(const f32x4*)src;
        a1 += *(const f32x4*)(src + 4);
      }
      u32x4 hq;
      hq[0] = pack_bf16(a0[0] + bf_lo(xres[0]), a0[1] + bf_hi(xres[0]));
      hq[1] = pack_bf16(a0[2] + bf_lo(xres[1]), a0[3] + bf_hi(xres[1]));
      hq[2] = pack_bf16(a1[0] + bf_lo(xres[2]), a1[1] + bf_hi(xres[2]));
      hq[3] = pack_bf16(a1[2] + bf_lo(xres[3]), a1[3] + bf_hi(xres[3]));
      float s1 = 0.f, s2 = 0.f, s3 = 0.f;
#pragma unroll
      for (int e = 0; e < 4; e++) {
        s1 = dot2bf(hq[e], ONES, s1);
        s2 = dot2bf(hq[e], hq[e], s2);
        s3 = dot2bf(hq[e], wrv[e], s3);
      }
      s1 = add_8(s1); s2 = add_8(s2); s3 = add_8(s3);
      if (cg == 0) st16(rs, WS_C1 + ((rb * 8 + ct) * 64 + tok) * 16, (u32x4){__float_as_uint(s1), __float_as_uint(s2), __float_as_uint(s3), tag});
      float wsum;
      {
        const u32x4 w4 = wr4;
        wsum = dot2bf(w4[0], ONES, 0.f); wsum = dot2bf(w4[1], ONES, wsum);
        wsum = dot2bf(w4[2], ONES, wsum); wsum = dot2bf(w4[3], ONES, wsum);
        wsum = add_64(wsum);
      }
      u32x4 g4;
#pragma unroll 1
      for (int it = 0; it < SPIN_MAX; it++) {
        g4 = ld16(rs, WS_C1 + ((rb * 8 + cg) * 64 + tok) * 16);
        if (__all(g4[3] == tag)) break;
        SPIN_PAUSE();
      }
      const float t1 = add_8(__uint_as_float(g4[0])), t2 = add_8(__uint_as_float(g4[1])), t3 = add_8(__uint_as_float(g4[2]));
      const float mean = t1 * (1.f / D_);
      const float rstd = rsqrtf(fmaxf(t2 * (1.f / D_) - mean * mean, 0.f) + LN_EPS);
      rv = rstd * (t3 - mean * wsum);
#pragma unroll
      for (int e = 0; e < 4; e++) {
        const float h0 = bf_lo(hq[e]), h1 = bf_hi(hq[e]);
        const float n0 = (h0 - mean) * rstd, n1 = (h1 - mean) * rstd;
        hn4[e] = pack_bf16(n0, n1);
        o1[2 * e] = h0 + n0; o1[2 * e + 1] = h1 + n1;
      }
      const float M1 = max_64(rv);
      unsigned long long bm = __ballot(rv == M1);
      const int t1i = __builtin_amdgcn_readlane(tok, bm ? __builtin_ctzll(bm) : 0);
      const float c2 = (tok == t1i) ? -INFINITY : rv;
      const float M2 = max_64(c2);
      bm = __ballot(c2 == M2 && tok != t1i);
      const int t2i = __builtin_amdgcn_readlane(tok, bm ? __builtin_ctzll(bm) : 0);
      if (lane == 0) *(f32x4*)(misc + 16 + wv * 4) = (f32x4){M1, __int_as_float(t1i), M2, __int_as_float(t2i)};
    }
    RAW_BAR();
    if (wv < 8) {
      const float cv = misc[16 + ((lane & 15) >> 1) * 4 + (lane & 1) * 2];
      const int ci = __float_as_int(misc[16 + ((lane & 15) >> 1) * 4 + (lane & 1) * 2 + 1]);
      const float M1 = max_16(cv);
      unsigned long long bm = __ballot(cv == M1);
      I1 = __builtin_amdgcn_readlane(ci, bm ? __builtin_ctzll(bm) : 0);
      const float c2 = (ci == I1) ? -INFINITY : cv;
      const float M2 = max_16(c2);
      bm = __ballot(c2 == M2 && ci != I1);
      I2 = __builtin_amdgcn_readlane(ci, bm ? __builtin_ctzll(bm) : 0);
      if (I2 == I1) I2 = I1 ^ 1;
      if (tok == I1 || tok == I2) {
        const int cd = rb * 2 + (tok == I2 ? 1 : 0);
        if (ct == 0) st16(rs, WS_C2 + cg * 1024 + cd * 16, (u32x4){__float_as_uint(rv), (unsigned)(rb * 64 + tok), 0u, tag});
        const unsigned xo = WS_XS + (cd * 256 + ct * 32 + cg * 4) * 8;
        st16(rs, xo, (u32x4){hn4[0], tag, hn4[1], tag});
        st16(rs, xo + 16, (u32x4){hn4[2], tag, hn4[3], tag});
        const unsigned oo = WS_O1 + (cd * 512 + ct * 64 + cg * 8) * 8;
#pragma unroll
        for (int e = 0; e < 4; e++)
          st16(rs, oo + e * 16, (u32x4){__float_as_uint(o1[2 * e]), tag, __float_as_uint(o1[2 * e + 1]), tag});
      } else {
        u32x4 ov;
#pragma unroll
        for (int e = 0; e < 4; e++) ov[e] = pack_bf16(o1[2 * e], o1[2 * e + 1]);
        *(u32x4*)(out + (size_t)(rb * 64 + tok) * D_ + ct * 64 + cg * 8) = ov;
      }
    } else if (wv == 8) {
      u32x4 g4;
#pragma unroll 1
      for (int it = 0; it < SPIN_MAX; it++) {
        g4 = ld16(rs, WS_C2 + xcd * 1024 + lane * 16);
        if (__all(g4[3] == tag)) break;
        SPIN_PAUSE();
      }
      const float cv = __uint_as_float(g4[0]);
      const float M1 = max_64(cv);
      unsigned long long bm = __ballot(cv == M1);
      const int l1 = bm ? __builtin_ctzll(bm) : 0;
      const float c2 = (lane == l1) ? -INFINITY : cv;
      const float M2 = max_64(c2);
      bm = __ballot(c2 == M2 && lane != l1);
      const int l2 = bm ? __builtin_ctzll(bm) : (l1 ^ 1);
      const int r1 = __builtin_amdgcn_readlane((int)g4[1], l1), r2 = __builtin_amdgcn_readlane((int)g4[1], l2);
      if (lane == 0) {
        *(f32x4*)(misc + 64) = (f32x4){__int_as_float(l1), __int_as_float(l2), __int_as_float(r1 & (S_ - 1)), __int_as_float(r2 & (S_ - 1))};
        misc[68] = M1; misc[69] = M2;
        if (b == 0) st4(rs, WS_EP, tag);
      }
    }
    if (wv >= 8) WAIT_VM(0);
    RAW_BAR();
  }
  const int cdA = __float_as_int(misc[64]), cdB = __float_as_int(misc[65]);
  const int rowA = __float_as_int(misc[66]), rowB = __float_as_int(misc[67]);
  if (wv < 8 && (tok == I1 || tok == I2)) {
    const int grow = rb * 64 + tok;
    if (grow != rowA && grow != rowB) {
      u32x4 ov;
#pragma unroll
      for (int e = 0; e < 4; e++) ov[e] = pack_bf16(o1[2 * e], o1[2 * e + 1]);
      *(u32x4*)(out + (size_t)grow * D_ + ct * 64 + cg * 8) = ov;
    }
  }
  if (is_up) {
    char* xs = smem + L_RED;
    if (wv >= 8 && wv < 12) {
      const int t = tid - 512, k = t >> 7, g2 = t & 127;
      const unsigned off = WS_XS + ((k ? cdB : cdA) * 256 + g2 * 2) * 8;
      u32x4 g4;
#pragma unroll 1
      for (int it = 0; it < SPIN_MAX; it++) {
        g4 = ld16(rs, off);
        if (__all(g4[1] == tag && g4[3] == tag)) break;
        SPIN_PAUSE();
      }
      *(u32x2*)(xs + k * 1024 + g2 * 8) = (u32x2){g4[0], g4[2]};
    }
    RAW_BAR();
    if (wv == 8) {
      const int g = lane >> 4, q = (lane & 15) >> 2, p = lane & 3;
      const char* xa = xs + (lane & 1) * 1024 + g * 16;
      const char* wa = smem + L_SC + (8 * g + q) * 32 + p * 8;
      f32x4 c0 = {0.f, 0.f, 0.f, 0.f}, c1 = {0.f, 0.f, 0.f, 0.f};
#pragma unroll
      for (int s = 0; s < 16; s += 2) {
        c0 = MFMA16(LD8(xa + s * 64), cat8(tr_read(wa + s * 1024), tr_read(wa + s * 1024 + 128)), c0);
        c1 = MFMA16(LD8(xa + s * 64 + 64), cat8(tr_read(wa + s * 1024 + 1024), tr_read(wa + s * 1024 + 1152)), c1);
      }
      const float g0 = c0[0] + c1[0], g1 = c0[1] + c1[1];
      const float u0 = dpp_f<0x128>(g0), u1 = dpp_f<0x128>(g1);
      if (lane < 8) {
        const unsigned gp = pack_bf16(g0, g1), up = pack_bf16(u0, u1);
        const float ga = bf_lo(gp), gb = bf_hi(gp);
        const unsigned sp = pack_bf16(ga / (1.f + __expf(-ga)), gb / (1.f + __expf(-gb)));
        const unsigned ap = pack_bf16(bf_lo(sp) * bf_lo(up), bf_hi(sp) * bf_hi(up));
        st8(rs, WS_DD + (cgp * 8 + lane) * 8, (u32x2){ap, tag});
      }
    }
  } else {
    unsigned short* act = (unsigned short*)(smem + L_RED);
    float* red2 = (float*)(smem + L_RED + 8192);
    float base = 0.f;
    if (wv == 15 && lane < 32) {
      const unsigned off = WS_O1 + (((lane >> 4) ? cdB : cdA) * 512 + dgp * 16 + (lane & 15)) * 8;
      u32x2 g2;
#pragma unroll 1
      for (int it = 0; it < SPIN_MAX; it++) {
        g2 = ld8(rs, off);
        if (__all(g2[1] == tag)) break;
        SPIN_PAUSE();
      }
      base = __uint_as_float(g2[0]);
    }
    if (wv < 14) {
      u32x4 g4;
#pragma unroll 1
      for (int it = 0; it < SPIN_MAX; it++) {
        g4 = ld16(rs, WS_DD + tid * 16);
        if (__all(g4[1] == tag && g4[3] == tag)) break;
        SPIN_PAUSE();
      }
      *(unsigned*)(act + tid * 2) = (g4[0] & 0xffffu) | (g4[2] << 16);
      *(unsigned*)(act + F_ + tid * 2) = (g4[0] >> 16) | (g4[2] & 0xffff0000u);
    }
    RAW_BAR();
    if (wv < 8) {
      const int g = lane >> 4, q = (lane & 15) >> 2, p = lane & 3;
      const char* xa = (const char*)act + (lane & 1) * (F_ * 2) + g * 16 + wv * 7 * 64;
      const char* wa = smem + L_RING + (wv * 7 * 32 + 8 * g + q) * 32 + p * 8;
      f32x4 c0 = {0.f, 0.f, 0.f, 0.f};
#pragma unroll
      for (int s = 0; s < 7; s++)
        c0 = MFMA16(LD8(xa + s * 64), cat8(tr_read(wa + s * 1024), tr_read(wa + s * 1024 + 128)), c0);
      if (lane < 16) { red2[wv * 32 + lane] = c0[0]; red2[wv * 32 + 16 + lane] = c0[1]; }
    }
    RAW_BAR();
    if (wv == 15 && lane < 32) {
      float y = 0.f;
#pragma unroll
      for (int k = 0; k < 8; k++) y += red2[k * 32 + lane];
      const float rsel = misc[68 + (lane >> 4)];
      const float gate = 1.f / (1.f + __expf(-rsel));
      const int grow = (lane >> 4) ? rowB : rowA;
      out[(size_t)grow * D_ + dgp * 16 + (lane & 15)] = (unsigned short)(pack_bf16(base + y * gate, 0.f) & 0xffffu);
    }
  }
}

extern "C" void kernel_launch(void* const* d_in, const int* in_sizes, int n_in,
                              void* d_out, int out_size, void* d_ws, size_t ws_size,
                              hipStream_t stream) {
  (void)hipFuncSetAttribute((const void*)fused_block, hipFuncAttributeMaxDynamicSharedMemorySize, L_TOTAL);
  fused_block<<<256, 1024, L_TOTAL, stream>>>((const unsigned short*)d_in[0], (const unsigned short*)d_in[1],
                                             (const unsigned short*)d_in[2], (const unsigned short*)d_in[4],
                                             (const unsigned short*)d_in[5], (const unsigned short*)d_in[6],
                                             (const unsigned short*)d_in[7], (char*)d_ws, (unsigned short*)d_out);
}
```
